```python
import torch

def _softmax(x, axis=-1):
    return torch.softmax(x, dim=axis)
def _gelu(x):
    return torch.nn.functional.gelu(x, approximate="tanh")
def _silu(x):
    return torch.nn.functional.silu(x)
def _sigmoid(x):
    return torch.sigmoid(x)
def _layernorm(x, axis=-1, eps=1e-5):
    m = x.mean(dim=axis, keepdim=True)
    v = x.var(dim=axis, keepdim=True, unbiased=False)
    return (x - m) / torch.sqrt(v + eps)
def _scatter_add(tgt, idx, src, axis=0):
    return tgt.clone().index_add_(axis, idx, src)
def _segment_sum(src, seg, n):
    out = torch.zeros((n,) + src.shape[1:], dtype=src.dtype)
    return out.index_add_(0, seg, src)
def _pad(x, pw):
    flat = tuple(v for pair in reversed(pw) for v in pair)
    return torch.nn.functional.pad(x, flat)

kernel_name = 'recurrent_scan__gqa_block_cached__dense_activation_gelu__decode'

def setup_inputs():

    _w = (lambda a: (lambda t: t.to(torch.bfloat16) if t.is_floating_point() else t)(torch.as_tensor(a)))
    def _A_setup():
        B, Sq, D = 8, 1, 448
        Hq, Hkv, Sk, Dh = 16, 8, 8192, 256
        assert Hq % Hkv == 0, (Hq, Hkv)
        return {
            "x": _w((lambda *s: torch.randn(*s, dtype=torch.bfloat16) * 0.05)(B, Sq, D)),
            "k_cache": _w((lambda *s: torch.randn(*s, dtype=torch.bfloat16) * 3.2)(B, Hkv, Sk, Dh)),
            "v_cache": _w((lambda *s: torch.randn(*s, dtype=torch.bfloat16) * 3.2)(B, Hkv, Sk, Dh)),
            "wq": _w((lambda *s: torch.randn(*s, dtype=torch.bfloat16) * 3.2)(D, Hq * Dh) * (D ** -0.5)),
            "wo": _w((lambda *s: torch.randn(*s, dtype=torch.bfloat16) * 3.2)(Hq * Dh, D) * ((Hq * Dh) ** -0.5)),
        }
    _A_in = _A_setup()

    def _B_setup():
        B, S, D, F = 8, 1, 448, 1792
        return {
            "x": _w((lambda *s: torch.randn(*s, dtype=torch.bfloat16) * 0.05)(B, S, D)),
            "w1": _w((lambda *s: torch.randn(*s, dtype=torch.bfloat16) * 3.2)(D, F) * (D ** -0.5)),
            "b1": _w((lambda *s: torch.randn(*s, dtype=torch.bfloat16) * 3.2)(F) * 0.01),
            "w2": _w((lambda *s: torch.randn(*s, dtype=torch.bfloat16) * 0.8)(F, D) * (F ** -0.5)),
            "b2": _w((lambda *s: torch.randn(*s, dtype=torch.bfloat16) * 0.8)(D) * 0.01),
        }
    _B_in = _B_setup()

    return {
        "A_x": _A_in["x"],
        "A_k_cache": _A_in["k_cache"],
        "A_v_cache": _A_in["v_cache"],
        "A_wq": _A_in["wq"],
        "A_wo": _A_in["wo"],
        "B_x": _B_in["x"],
        "B_w1": _B_in["w1"],
        "B_b1": _B_in["b1"],
        "B_w2": _B_in["w2"],
        "B_b2": _B_in["b2"],
    }

def reference(A_x, A_k_cache, A_v_cache, A_wq, A_wo, B_x, B_w1, B_b1, B_w2, B_b2):
    def _A_ref(x, k_cache, v_cache, wq, wo):
        B, Sq, D = x.shape
        Hq, Dh = 16, 256
        Hkv = k_cache.shape[1]
        rep = Hq // Hkv
        q = (lambda x, *d: x.permute(*d))((x @ wq).reshape(B, Sq, Hq, Dh), 0, 2, 1, 3)
        k = (lambda x, n, axis: torch.repeat_interleave(x, n, dim=axis))(k_cache, rep, axis=1)
        v = (lambda x, n, axis: torch.repeat_interleave(x, n, dim=axis))(v_cache, rep, axis=1)
        scores = torch.einsum("bhqd,bhkd->bhqk", q, k) * (Dh ** -0.5)
        p = _softmax(scores, axis=-1)
        o = torch.einsum("bhqk,bhkd->bhqd", p, v)
        return (lambda x, *d: x.permute(*d))(o, 0, 2, 1, 3).reshape(B, Sq, Hq * Dh) @ wo

    def _B_ref(x, w1, b1, w2, b2):
        h = torch.einsum("bsd,df->bsf", x, w1) + b1
        h = _gelu(h)
        return torch.einsum("bsf,fd->bsd", h, w2) + b2

    x = A_x
    xn = _layernorm(x, axis=-1)
    s = _A_ref(xn, A_k_cache, A_v_cache, A_wq, A_wo)
    g = _silu(_B_ref(xn, B_w1, B_b1, B_w2, B_b2))
    return x + s * g
```

```cpp
#include <hip/hip_runtime.h>
#include <hip/hip_bf16.h>
#include <stdint.h>

#define MIMIC 0

typedef unsigned short u16;
typedef unsigned v4u __attribute__((ext_vector_type(4)));
typedef __bf16 bf16x2 __attribute__((ext_vector_type(2)));

constexpr int B = 8, D = 448, HQ = 16, HKV = 8, SK = 8192, DH = 256, F = 1792;
constexpr int NSPLIT = 4;

__device__ __forceinline__ float bf2f(u16 b) { return __uint_as_float(((unsigned)b) << 16); }
__device__ __forceinline__ float rbf(float f) {
  unsigned u = __float_as_uint(f); u += 0x7FFFu + ((u >> 16) & 1u); return __uint_as_float(u & 0xffff0000u); }
__device__ __forceinline__ u16 f2bf(float f) { unsigned u = __float_as_uint(f); u += 0x7FFFu + ((u >> 16) & 1u); return (u16)(u >> 16); }
__device__ __forceinline__ float R(float f) { return MIMIC ? rbf(f) : f; }

__device__ void layernorm_all(const u16* x, float* xn  ) {
  int wave = threadIdx.x >> 6, lane = threadIdx.x & 63;
  for (int rr = 0; rr < 2; ++rr) {
    int b = wave * 2 + rr;
    float v[7]; float s = 0.f;
    for (int i = 0; i < 7; ++i) { v[i] = bf2f(x[b * D + lane + 64 * i]); s += v[i]; }
    for (int m = 32; m >= 1; m >>= 1) s += __shfl_xor(s, m);
    float mean = R(s / D);
    float mean_v = s / D;
    float q = 0.f;
    for (int i = 0; i < 7; ++i) { float d = v[i] - mean_v; q += d * d; }
    for (int m = 32; m >= 1; m >>= 1) q += __shfl_xor(q, m);
    float var = R(q / D);
    float den = R(sqrtf(R(var + 1e-5f)));
    for (int i = 0; i < 7; ++i) xn[b * D + lane + 64 * i] = R(R(v[i] - mean) / den);
  }
}

__global__ __launch_bounds__(256) void k_prep(const u16* x, const u16* wq, const u16* w1, const u16* b1, float* q, float* h) {
  __shared__ float xn[B * D];
  __shared__ float red[4][8][64];
  layernorm_all(x, xn);
  __syncthreads();
  int wave = threadIdx.x >> 6, lane = threadIdx.x & 63;
  bool isq = blockIdx.x < 64;
  const u16* W = isq ? wq : w1; int ld = isq ? HQ * DH : F; int col = (isq ? blockIdx.x : blockIdx.x - 64) * 64 + lane;
  float acc[8] = {0, 0, 0, 0, 0, 0, 0, 0};
  for (int k = wave; k < D; k += 4) {
    float w = bf2f(W[(size_t)k * ld + col]);
    for (int b = 0; b < 8; ++b) acc[b] += xn[b * D + k] * w;
  }
  for (int b = 0; b < 8; ++b) red[wave][b][lane] = acc[b];
  __syncthreads();
  for (int b = wave * 2; b < wave * 2 + 2; ++b) {
    float s = red[0][b][lane] + red[1][b][lane] + red[2][b][lane] + red[3][b][lane];
    if (isq) q[b * HQ * DH + col] = R(s);
    else {
      float t = R(R(s) + bf2f(b1[col]));
      float u = 0.7978845608028654f * (t + 0.044715f * t * t * t);
      float g = 0.5f * t * (1.f + tanhf(u));
      h[b * F + col] = R(g);
    }
  }
}

__global__ __launch_bounds__(256) void k_attn(const u16* kc, const u16* vc, const float* q, float* pm, float* pl, float* po) {
  constexpr int KEYS = SK / NSPLIT;
  __shared__ float sc[2][KEYS];
  __shared__ float qs[2][DH];
  __shared__ float red[8];
  __shared__ float accs[4][2][DH];
  int sp = blockIdx.x, kvh = blockIdx.y, b = blockIdx.z;
  int tid = threadIdx.x, wave = tid >> 6, lane = tid & 63;
  for (int i = tid; i < 512; i += 256) qs[i >> 8][i & 255] = q[b * HQ * DH + kvh * 512 + i];
  __syncthreads();
  const u16* kb = kc + ((size_t)(b * HKV + kvh) * SK + (size_t)sp * KEYS) * DH;
  const u16* vb = vc + ((size_t)(b * HKV + kvh) * SK + (size_t)sp * KEYS) * DH;
  int c = lane & 31, r = lane >> 5;
  float q0[8], q1[8];
  for (int j = 0; j < 8; ++j) { q0[j] = qs[0][c * 8 + j]; q1[j] = qs[1][c * 8 + j]; }
  for (int key = wave * 2 + r; key < KEYS; key += 8) {
    v4u kv = *(const v4u*)(kb + (size_t)key * DH + c * 8);
    float s0 = 0.f, s1 = 0.f;
    for (int j = 0; j < 4; ++j) {
      float lo = __uint_as_float(kv[j] << 16), hi = __uint_as_float(kv[j] & 0xffff0000u);
      s0 += lo * q0[2 * j] + hi * q0[2 * j + 1]; s1 += lo * q1[2 * j] + hi * q1[2 * j + 1];
    }
    for (int m = 16; m >= 1; m >>= 1) { s0 += __shfl_xor(s0, m); s1 += __shfl_xor(s1, m); }
    if (c == 0) { sc[0][key] = R(s0) * 0.0625f; sc[1][key] = R(s1) * 0.0625f; }
  }
  __syncthreads();
  float mx[2], sum[2];
  for (int hh = 0; hh < 2; ++hh) {
    float m = -INFINITY;
    for (int i = tid; i < KEYS; i += 256) m = fmaxf(m, sc[hh][i]);
    for (int o = 32; o >= 1; o >>= 1) m = fmaxf(m, __shfl_xor(m, o));
    if (lane == 0) red[wave] = m;
    __syncthreads();
    m = fmaxf(fmaxf(red[0], red[1]), fmaxf(red[2], red[3]));
    __syncthreads();
    float s = 0.f;
    for (int i = tid; i < KEYS; i += 256) { float p = __expf(sc[hh][i] - m); sc[hh][i] = p; s += p; }
    for (int o = 32; o >= 1; o >>= 1) s += __shfl_xor(s, o);
    if (lane == 0) red[wave] = s;
    __syncthreads();
    s = red[0] + red[1] + red[2] + red[3];
    __syncthreads();
    mx[hh] = m; sum[hh] = s;
  }
  float a0[8] = {0}, a1[8] = {0};
  for (int key = wave * 2 + r; key < KEYS; key += 8) {
    v4u vv = *(const v4u*)(vb + (size_t)key * DH + c * 8);
    float p0 = sc[0][key], p1 = sc[1][key];
    for (int j = 0; j < 4; ++j) {
      float lo = __uint_as_float(vv[j] << 16), hi = __uint_as_float(vv[j] & 0xffff0000u);
      a0[2 * j] += p0 * lo; a0[2 * j + 1] += p0 * hi; a1[2 * j] += p1 * lo; a1[2 * j + 1] += p1 * hi;
    }
  }
  for (int j = 0; j < 8; ++j) { a0[j] += __shfl_xor(a0[j], 32); a1[j] += __shfl_xor(a1[j], 32); }
  if (r == 0) for (int j = 0; j < 8; ++j) { accs[wave][0][c * 8 + j] = a0[j]; accs[wave][1][c * 8 + j] = a1[j]; }
  __syncthreads();
  for (int i = tid; i < 512; i += 256) {
    int hh = i >> 8, d = i & 255;
    float s = accs[0][hh][d] + accs[1][hh][d] + accs[2][hh][d] + accs[3][hh][d];
    po[(((size_t)b * HQ + kvh * 2 + hh) * NSPLIT + sp) * DH + d] = s;
  }
  if (tid < 2) { pm[(b * HQ + kvh * 2 + tid) * NSPLIT + sp] = mx[tid]; pl[(b * HQ + kvh * 2 + tid) * NSPLIT + sp] = sum[tid]; }
}

__global__ __launch_bounds__(256) void k_final(const u16* x, const u16* wo, const u16* w2, const u16* b2, const float* h,
                                               const float* pm, const float* pl, const float* po, u16* out) {
  __shared__ float o[HQ * DH];
  __shared__ float hs[F];
  __shared__ float red[2][4][64];
  int b = blockIdx.y, cg = blockIdx.x;
  int tid = threadIdx.x, wave = tid >> 6, lane = tid & 63;
  for (int i = tid; i < HQ * DH; i += 256) {
    int hq = i >> 8, d = i & 255;
    float M = -INFINITY;
    for (int s = 0; s < NSPLIT; ++s) M = fmaxf(M, pm[(b * HQ + hq) * NSPLIT + s]);
    float L = 0.f, a = 0.f;
    for (int s = 0; s < NSPLIT; ++s) { float w = __expf(pm[(b * HQ + hq) * NSPLIT + s] - M); L += w * pl[(b * HQ + hq) * NSPLIT + s]; a += w * po[(((size_t)b * HQ + hq) * NSPLIT + s) * DH + d]; }
    o[i] = R(a / L);
  }
  for (int i = tid; i < F; i += 256) hs[i] = h[b * F + i];
  __syncthreads();
  int col = cg * 64 + lane;
  float s = 0.f, g = 0.f;
  for (int k = wave; k < HQ * DH; k += 4) s += o[k] * bf2f(wo[(size_t)k * D + col]);
  for (int k = wave; k < F; k += 4) g += hs[k] * bf2f(w2[(size_t)k * D + col]);
  red[0][wave][lane] = s; red[1][wave][lane] = g;
  __syncthreads();
  if (wave == 0) {
    s = R(red[0][0][lane] + red[0][1][lane] + red[0][2][lane] + red[0][3][lane]);
    g = red[1][0][lane] + red[1][1][lane] + red[1][2][lane] + red[1][3][lane];
    g = R(R(g) + bf2f(b2[col]));
    float sg = R(g / (1.f + __expf(-g)));
    float r = bf2f(x[b * D + col]) + R(s * sg);
    out[b * D + col] = f2bf(r);
  }
}

extern "C" void kernel_launch(void* const* d_in, const int* in_sizes, int n_in, void* d_out, int out_size, void* d_ws, size_t ws_size, hipStream_t stream) {
  const u16* x = (const u16*)d_in[0]; const u16* kc = (const u16*)d_in[1]; const u16* vc = (const u16*)d_in[2];
  const u16* wq = (const u16*)d_in[3]; const u16* wo = (const u16*)d_in[4];
  const u16* w1 = (const u16*)d_in[6]; const u16* b1 = (const u16*)d_in[7]; const u16* w2 = (const u16*)d_in[8]; const u16* b2 = (const u16*)d_in[9];
  float* ws = (float*)d_ws;
  float* q = ws; float* h = q + B * HQ * DH; float* pm = h + B * F; float* pl = pm + B * HQ * NSPLIT; float* po = pl + B * HQ * NSPLIT;
  k_prep<<<92, 256, 0, stream>>>(x, wq, w1, b1, q, h);
  k_attn<<<dim3(NSPLIT, HKV, B), 256, 0, stream>>>(kc, vc, q, pm, pl, po);
  k_final<<<dim3(7, B), 256, 0, stream>>>(x, wo, w2, b2, h, pm, pl, po, (u16*)d_out);
}
```

```cpp
#include <hip/hip_runtime.h>
#include <hip/hip_bf16.h>
#include <stdint.h>

typedef unsigned short u16;
typedef unsigned v4u __attribute__((ext_vector_type(4)));
typedef unsigned v2u __attribute__((ext_vector_type(2)));
typedef float f32x4 __attribute__((ext_vector_type(4)));
typedef short bf16x8 __attribute__((ext_vector_type(8)));
typedef __bf16 bf16x2 __attribute__((ext_vector_type(2)));
typedef unsigned long long u64;
#define LAS __attribute__((address_space(3)))
#define GAS __attribute__((address_space(1)))
#define RLX_AGENT __ATOMIC_RELAXED, __HIP_MEMORY_SCOPE_AGENT

constexpr int D = 448, NQ = 4096, SK = 8192, F = 1792;
#define AUXPOL 2
constexpr int NBATCH = 32;
constexpr float L2E = 1.4426950408889634f;
constexpr int NTHREADS = 640;

constexpr unsigned WS_EPOCH = 0;
constexpr unsigned WS_QBOX = 4096;
constexpr unsigned WS_PBOX = WS_QBOX + 8 * 8 * 32 * 64;
constexpr unsigned PMSG = 136 * 8;
constexpr unsigned WS_FS = WS_PBOX + 64 * 16 * PMSG;
constexpr unsigned WS_FH = WS_FS + 8 * 32 * 32 * 128;
constexpr unsigned WS_END = WS_FH + 8 * 32 * 32 * 128;

constexpr int L_WO = 0;
constexpr int SC_STRIDE = 8 * 68;
constexpr int L_SC = L_WO + 128 * 896;
constexpr int L_XN = L_SC + 8 * SC_STRIDE * 4;
constexpr int L_WQ = L_XN + 8 * 896;
constexpr int L_QV = L_WQ + 448 * 32;
constexpr int L_TAG = L_QV + 1024, L_QS = L_TAG + 16, L_OS = L_QS + 256, L_G = L_OS + 512, L_HRED = L_G + 64, L_HS = L_HRED + 2016, L_ML = L_HS + 224;
constexpr int L_END = L_ML + 128;
static_assert(L_END <= 160 * 1024, "lds");

__device__ __forceinline__ float bf2f(u16 b) { return __uint_as_float(((unsigned)b) << 16); }
__device__ __forceinline__ float bflo(unsigned w) { return __uint_as_float(w << 16); }
__device__ __forceinline__ float bfhi(unsigned w) { return __uint_as_float(w & 0xffff0000u); }
__device__ __forceinline__ unsigned pk2(float a, float b) { bf16x2 v = {(__bf16)a, (__bf16)b}; return __builtin_bit_cast(unsigned, v); }
__device__ __forceinline__ float rbf(float f) { return bflo(pk2(f, f)); }
__device__ __forceinline__ float dot2(unsigned k, unsigned q, float acc) {
  return __builtin_amdgcn_fdot2_f32_bf16(__builtin_bit_cast(bf16x2, k), __builtin_bit_cast(bf16x2, q), acc, false);
}
template <int CTRL> __device__ __forceinline__ float dpp(float x) {
  return __builtin_bit_cast(float, __builtin_amdgcn_mov_dpp(__builtin_bit_cast(int, x), CTRL, 0xf, 0xf, true));
}
constexpr int XOR1 = 0xB1, XOR2 = 0x4E, HMIR = 0x141, ROR8 = 0x128;
__device__ __forceinline__ float sum16(float x) {
  auto s = __builtin_amdgcn_permlane16_swap(__float_as_uint(x), __float_as_uint(x), false, false); return __uint_as_float(s[0]) + __uint_as_float(s[1]); }
__device__ __forceinline__ float sum32(float x) {
  auto s = __builtin_amdgcn_permlane32_swap(__float_as_uint(x), __float_as_uint(x), false, false); return __uint_as_float(s[0]) + __uint_as_float(s[1]); }
__device__ __forceinline__ float max16(float x) {
  auto s = __builtin_amdgcn_permlane16_swap(__float_as_uint(x), __float_as_uint(x), false, false); return fmaxf(__uint_as_float(s[0]), __uint_as_float(s[1])); }
__device__ __forceinline__ float max32(float x) {
  auto s = __builtin_amdgcn_permlane32_swap(__float_as_uint(x), __float_as_uint(x), false, false); return fmaxf(__uint_as_float(s[0]), __uint_as_float(s[1])); }
__device__ __forceinline__ float wsum64(float x) {
  x += dpp<XOR1>(x); x += dpp<XOR2>(x); x += dpp<HMIR>(x); x += dpp<ROR8>(x); x = sum16(x); return sum32(x); }
__device__ __forceinline__ f32x4 mfma(v4u a, v4u b, f32x4 c) {
  return __builtin_amdgcn_mfma_f32_16x16x32_bf16(__builtin_bit_cast(bf16x8, a), __builtin_bit_cast(bf16x8, b), c, 0, 0, 0);
}
__device__ __forceinline__ u64 rt() { return __builtin_amdgcn_s_memrealtime(); }
#define WAITV(n) asm volatile("s_waitcnt vmcnt(" #n ")" ::: "memory")
#define WAITL() asm volatile("s_waitcnt lgkmcnt(0)" ::: "memory")
#define SB() __builtin_amdgcn_sched_barrier(0)
#define BAR() do { WAITL(); __builtin_amdgcn_s_barrier(); asm volatile("" ::: "memory"); } while (0)
__device__ __forceinline__ void st_gran(GAS u64* p, unsigned tag, unsigned val) { __hip_atomic_store(p, ((u64)tag << 32) | val, RLX_AGENT); }
__device__ __forceinline__ u64 ld_gran(GAS u64* p) { return __hip_atomic_load(p, RLX_AGENT); }
constexpr u64 TMO = 400000;
constexpr unsigned OOB = 0x7fffff00u;

#define STP(k) do {} while (0)

__global__ __launch_bounds__(NTHREADS) void fused(const u16* __restrict__ x, const u16* __restrict__ kc, const u16* __restrict__ vc, const u16* __restrict__ wq, const u16* __restrict__ wo,
                                             const u16* __restrict__ w1, const u16* __restrict__ b1, const u16* __restrict__ w2, const u16* __restrict__ b2, u16* __restrict__ out, unsigned char* __restrict__ ws) {
  extern __shared__ __attribute__((aligned(16))) unsigned char lds_[];
  LAS unsigned char* lds = (LAS unsigned char*)lds_;
  const int bid = blockIdx.x; const int b = bid >> 5, j32 = bid & 31, kvh = j32 >> 2, sp = bid & 3;
  const int tid = threadIdx.x, lane = tid & 63; const int w = __builtin_amdgcn_readfirstlane(tid >> 6);
  GAS unsigned char* wsg = (GAS unsigned char*)ws;
  STP(0);
  const u64 tstart = rt();
  unsigned tag = 0;
  const size_t kvbase = ((size_t)(b * 8 + kvh) * SK + (size_t)sp * 2048) * 512;
  float o_own = 0.f, M_own = 0.f, L_own = 0.f;

  if (w < 8) {
    const __amdgpu_buffer_rsrc_t rk = __builtin_amdgcn_make_buffer_rsrc((void*)((const char*)kc + kvbase), 0, 1u << 20, 0x00020000);
    const __amdgpu_buffer_rsrc_t rv = __builtin_amdgcn_make_buffer_rsrc((void*)((const char*)vc + kvbase), 0, 1u << 20, 0x00020000);
    const __amdgpu_buffer_rsrc_t rx = __builtin_amdgcn_make_buffer_rsrc((void*)x, 0, 8 * 896, 0x00020000);
    const unsigned koff = lane * 16;
    const unsigned voff = (lane >> 4) * 512 + (lane & 15) * 16;
    auto ldk = [&](int i, v4u (&r)[4]) {
#pragma unroll
      for (int u = 0; u < 4; ++u) r[u] = __builtin_amdgcn_raw_buffer_load_b128(rk, koff + u * 1024, (unsigned)(i * 8 + w) * 4096u, 2);
    };
    auto ldv = [&](int i, v4u (&r)[4]) {
#pragma unroll
      for (int u = 0; u < 4; ++u) r[u] = __builtin_amdgcn_raw_buffer_load_b128(rv, voff + (u >> 1) * 2048 + (u & 1) * 256, (unsigned)(i * 8 + w) * 4096u, 2);
    };
    v4u K0[4], K1[4], K2[4], K3[4];
    const v4u xr = __builtin_amdgcn_raw_buffer_load_b128(rx, lane < 56 ? (unsigned)(w * 896 + lane * 16) : OOB, 0, 0);
    SB();
    ldk(0, K0); SB();
    {
      float v[8];
#pragma unroll
      for (int k = 0; k < 4; ++k) { v[2 * k] = bflo(xr[k]); v[2 * k + 1] = bfhi(xr[k]); }
      float s = ((v[0] + v[1]) + (v[2] + v[3])) + ((v[4] + v[5]) + (v[6] + v[7]));
      s = wsum64(s);
      const float mean = s * (1.0f / 448.0f);
      float q = 0.f;
#pragma unroll
      for (int k = 0; k < 8; ++k) { const float d = v[k] - mean; q += d * d; }
      q = lane < 56 ? q : 0.f;
      q = wsum64(q);
      const float meanb = rbf(mean);
      const float den = rbf(sqrtf(rbf(rbf(q * (1.0f / 448.0f)) + 1e-5f)));
      float rc = __builtin_amdgcn_rcpf(den); rc = rc * (2.0f - den * rc);
      v4u o;
#pragma unroll
      for (int k = 0; k < 4; ++k) { const unsigned d2 = pk2(v[2 * k] - meanb, v[2 * k + 1] - meanb); o[k] = pk2(bflo(d2) * rc, bfhi(d2) * rc); }
      if (lane < 56) *(LAS v4u*)(lds + L_XN + w * 896 + lane * 16) = o;
    }
    STP(8);
    BAR();
    WAITV(0); SB(); ldk(1, K1); SB();
    WAITV(0); SB(); ldk(2, K2); SB();
    WAITV(0); SB(); ldk(3, K3); SB();
    STP(1);
    BAR();
    STP(2);
    const int c = lane & 31;
    const v4u q0 = *(const LAS v4u*)(lds + L_QV + c * 16), q1 = *(const LAS v4u*)(lds + L_QV + 512 + c * 16);
    tag = *(const LAS unsigned*)(lds + L_TAG);
    LAS float* scw = (LAS float*)(lds + L_SC) + w * SC_STRIDE;
    float mrun = -INFINITY;
    const int hsel = (lane >> 2) & 1, ksel = 2 * ((lane >> 3) & 3) + (lane >> 5);
    auto kcomp = [&](int i, const v4u (&r)[4]) {
      float xx[8];
#pragma unroll
      for (int u = 0; u < 4; ++u) {
        float a = 0.f, bb = 0.f;
#pragma unroll
        for (int k = 0; k < 4; ++k) { a = dot2(r[u][k], q0[k], a); bb = dot2(r[u][k], q1[k], bb); }
        xx[2 * u] = a; xx[2 * u + 1] = bb;
      }
      float y[4];
#pragma unroll
      for (int v = 0; v < 4; ++v) {
        auto s = __builtin_amdgcn_permlane16_swap(__float_as_uint(xx[v]), __float_as_uint(xx[v + 4]), false, false);
        y[v] = __uint_as_float(s[0]) + __uint_as_float(s[1]);
      }
      float z[2];
#pragma unroll
      for (int v = 0; v < 2; ++v) {
        const float t1 = y[v] + dpp<ROR8>(y[v]), t2 = y[v + 2] + dpp<ROR8>(y[v + 2]);
        z[v] = (lane & 8) ? t2 : t1;
      }
      const float t1 = z[0] + dpp<HMIR>(z[0]), t2 = z[1] + dpp<HMIR>(z[1]);
      float s = (lane & 4) ? t2 : t1;
      s += dpp<XOR2>(s); s += dpp<XOR1>(s);
      s = rbf(s) * 0.0625f;
      mrun = fmaxf(mrun, s);
      if ((lane & 3) == 0) scw[hsel * 256 + i * 8 + ksel] = s;
    };
    kcomp(0, K0); SB(); kcomp(1, K1); SB(); kcomp(2, K2); SB();
#pragma unroll 1
    for (int i = 3; i < NBATCH - 1; i += 2) {
      WAITV(0); SB();
      ldk(i + 1, K0); SB();
      kcomp(i, K3); SB();
      WAITV(0); SB();
      ldk(i + 2, K3); SB();
      kcomp(i + 1, K0); SB();
    }
    WAITV(0); SB();
    ldv(0, K0); SB();
    kcomp(NBATCH - 1, K3); SB();
    STP(3);
    mrun = fmaxf(mrun, dpp<ROR8>(mrun)); mrun = max16(mrun); mrun = max32(mrun);
    const float m0 = __builtin_bit_cast(float, __builtin_amdgcn_readlane(__builtin_bit_cast(int, mrun), 0));
    const float m1 = __builtin_bit_cast(float, __builtin_amdgcn_readlane(__builtin_bit_cast(int, mrun), 4));
    const int hh = lane >> 5;
    const float mh = hh ? m1 : m0;
    float lsum;
    {
      WAITL();
      const f32x4 s0 = *(LAS f32x4*)(scw + hh * 256 + c * 8), s1 = *(LAS f32x4*)(scw + hh * 256 + c * 8 + 4);
      WAITL();
      unsigned pw[8]; lsum = 0.f;
#pragma unroll
      for (int e = 0; e < 4; ++e) {
        const float p0 = __builtin_amdgcn_exp2f((s0[e] - mh) * L2E), p1 = __builtin_amdgcn_exp2f((s1[e] - mh) * L2E);
        lsum += p0 + p1;
        pw[e] = pk2(p0, p0); pw[e + 4] = pk2(p1, p1);
      }
      LAS unsigned* pwl = (LAS unsigned*)scw;
#pragma unroll
      for (int g = 0; g < 4; ++g) *(LAS v2u*)(pwl + (hh * 4 + g) * 68 + 2 * c) = v2u{pw[g], pw[g + 4]};
      lsum += dpp<XOR1>(lsum); lsum += dpp<XOR2>(lsum); lsum += dpp<HMIR>(lsum); lsum += dpp<ROR8>(lsum); lsum = sum16(lsum);
      WAITL();
    }
    f32x4 acc0 = {0, 0, 0, 0}, acc1 = {0, 0, 0, 0};
    const int ii = lane & 15, g = lane >> 4, hV = ii >> 3, jj = ii & 7;
    unsigned msk[4];
#pragma unroll
    for (int r = 0; r < 4; ++r) msk[r] = (r == (jj >> 1)) ? ((jj & 1) ? 0xffff0000u : 0x0000ffffu) : 0u;
    const LAS unsigned* prd = (const LAS unsigned*)scw + (hV * 4 + g) * 68;
    auto vcomp = [&](int i, const v4u (&r)[4]) {
      const v2u pw = *(const LAS v2u*)(prd + 2 * i);
#pragma unroll
      for (int rg = 0; rg < 2; ++rg) {
        const unsigned pv = pw[rg];
        const v4u a = {pv & msk[0], pv & msk[1], pv & msk[2], pv & msk[3]};
        acc0 = mfma(a, r[2 * rg], acc0);
        acc1 = mfma(a, r[2 * rg + 1], acc1);
      }
    };
#pragma unroll 1
    for (int i = 0; i < NBATCH; i += 2) {
      WAITV(0); SB();
      ldv(i + 1, K3); SB();
      vcomp(i, K0); SB();
      WAITV(0); SB();
      if (i + 2 < NBATCH) ldv(i + 2, K0);
      SB();
      vcomp(i + 1, K3); SB();
    }
    STP(4);
    LAS float* accw = (LAS float*)(lds + L_SC) + w * SC_STRIDE + hh * 256 + 8 * (lane & 15) + 4 * ((lane >> 4) & 1);
    *(LAS f32x4*)(accw) = acc0; *(LAS f32x4*)(accw + 128) = acc1;
    LAS float* ml = (LAS float*)(lds + L_ML) + w * 4;
    if (c == 0) { ml[hh * 2] = mh; ml[hh * 2 + 1] = lsum; }
  } else if (w == 8) {
    const unsigned E = __hip_atomic_load((GAS unsigned*)(wsg + WS_EPOCH), RLX_AGENT);
    const int pidx = sp * 8 + b;
    {
      const u16* src = wq + (size_t)(lane >> 1) * NQ + kvh * 512 + pidx * 16 + (lane & 1) * 8;
#pragma unroll
      for (int it = 0; it < 14; ++it)
        __builtin_amdgcn_global_load_lds((const unsigned*)(src + (size_t)it * 32 * NQ), (unsigned*)(lds_ + L_WQ + it * 1024), 16, 0, AUXPOL);
    }
    unsigned warm;
    {
      const GAS unsigned char* p;
      if (lane < 16) p = (const GAS unsigned char*)vc + kvbase + (size_t)lane * 65536;
      else if (lane < 32) p = (const GAS unsigned char*)kc + kvbase + (size_t)(lane - 16) * 65536;
      else if (lane < 40) p = wsg + WS_PBOX + (size_t)(b * 8 + kvh) * 16 * PMSG + (lane - 32) * 2176;
      else if (lane < 48) p = wsg + WS_FS + (size_t)b * 131072 + (lane - 40) * 16384;
      else if (lane < 56) p = wsg + WS_FH + (size_t)b * 131072 + (lane - 48) * 16384;
      else p = (const GAS unsigned char*)wo + ((size_t)(kvh * 512 + sp * 128)) * 896 + (lane - 56) * 14336;
      warm = __builtin_nontemporal_load((const GAS unsigned*)p);
    }
    tag = E * 0x9E3779B1u + 0x7F4A7C15u;
    BAR();
    WAITV(1);
    STP(10);
    {
      const int i16 = lane & 15, g = lane >> 4;
      const unsigned aaddr = L_XN + (i16 & 7) * 896 + g * 16;
      const unsigned baddr = (unsigned)(size_t)(lds) + L_WQ + (8 * g + (i16 >> 2)) * 32 + (i16 & 3) * 8;
      v4u a[14]; v2u b0[14], b1v[14];
#pragma unroll
      for (int ks = 0; ks < 14; ++ks) a[ks] = *(const LAS v4u*)(lds + aaddr + ks * 64);
#pragma unroll
      for (int ks = 0; ks < 14; ++ks)
        asm volatile("ds_read_b64_tr_b16 %0, %2 offset:%3\n\tds_read_b64_tr_b16 %1, %2 offset:%4" : "=&v"(b0[ks]), "=&v"(b1v[ks]) : "v"(baddr), "i"(ks * 1024), "i"(ks * 1024 + 128) : "memory");
      asm volatile("s_waitcnt lgkmcnt(0)" : "+v"(b0[0]), "+v"(b0[1]), "+v"(b0[2]), "+v"(b0[3]), "+v"(b0[4]), "+v"(b0[5]), "+v"(b0[6]), "+v"(b0[7]), "+v"(b0[8]), "+v"(b0[9]), "+v"(b0[10]), "+v"(b0[11]), "+v"(b0[12]), "+v"(b0[13]) :: "memory");
      asm volatile("" : "+v"(b1v[0]), "+v"(b1v[1]), "+v"(b1v[2]), "+v"(b1v[3]), "+v"(b1v[4]), "+v"(b1v[5]), "+v"(b1v[6]), "+v"(b1v[7]), "+v"(b1v[8]), "+v"(b1v[9]), "+v"(b1v[10]), "+v"(b1v[11]), "+v"(b1v[12]), "+v"(b1v[13]) :: "memory");
      SB();
      f32x4 dq0 = {0, 0, 0, 0}, dq1 = {0, 0, 0, 0};
#pragma unroll
      for (int ks = 0; ks < 14; ks += 2) {
        dq0 = mfma(a[ks], v4u{b0[ks][0], b0[ks][1], b1v[ks][0], b1v[ks][1]}, dq0);
        dq1 = mfma(a[ks + 1], v4u{b0[ks + 1][0], b0[ks + 1][1], b1v[ks + 1][0], b1v[ks + 1][1]}, dq1);
      }
      const f32x4 dq = dq0 + dq1;
      LAS u16* qs = (LAS u16*)(lds + L_QS);
      if (g < 2) {
#pragma unroll
        for (int r = 0; r < 4; ++r) qs[(4 * g + r) * 16 + i16] = (u16)(pk2(dq[r], dq[r]) & 0xffffu);
      }
      WAITL();
      const unsigned qw = *(const LAS unsigned*)(lds + L_QS + lane * 4);
      st_gran((GAS u64*)(wsg + WS_QBOX + (((kvh * 8 + (lane >> 3)) * 32 + pidx) * 8 + (lane & 7)) * 8), tag, qw);
    }
    STP(1);
    {
      const __amdgpu_buffer_rsrc_t rq = __builtin_amdgcn_make_buffer_rsrc((void*)(ws + WS_QBOX + (kvh * 8 + b) * 2048), 0, 2048, 0x00020000);
      v4u g0, g1;
      for (;;) {
        asm volatile("" ::: "memory");
        g0 = __builtin_amdgcn_raw_buffer_load_b128(rq, lane * 32, 0, 16);
        g1 = __builtin_amdgcn_raw_buffer_load_b128(rq, lane * 32 + 16, 0, 16);
        const bool ok = g0[1] == tag && g0[3] == tag && g1[1] == tag && g1[3] == tag;
        if (__all(ok)) break;
        if (rt() - tstart > TMO) break;
      }
      *(LAS v4u*)(lds + L_QV + lane * 16) = v4u{g0[0], g0[2], g1[0], g1[2]};
      if (lane == 0) *(LAS unsigned*)(lds + L_TAG) = tag;
    }
    STP(2);
    BAR();
    asm volatile("" :: "v"(warm));
    {
      GAS u64* fb = (GAS u64*)(wsg + WS_FH + (b * 32 + j32) * 4096) + lane;
      float s = 0.f;
      for (;;) {
        __builtin_amdgcn_s_sleep(64);
        asm volatile("" ::: "memory");
        bool ok = true; s = 0.f;
        u64 gv[8];
#pragma unroll
        for (int k = 0; k < 8; ++k) gv[k] = ld_gran(fb + k * 64);
#pragma unroll
        for (int k = 0; k < 8; ++k) { ok &= ((unsigned)(gv[k] >> 32) == tag) || ((lane & 15) >= 14); s += __uint_as_float((unsigned)gv[k]); }
        if (__all(ok)) break;
        if (rt() - tstart > TMO) break;
      }
      s = sum16(s); s = sum32(s);
      if (lane < 14) {
        const int col = j32 * 14 + lane;
        const float gg = rbf(rbf(s) + bf2f(b2[col]));
        const float sg = rbf(gg / (1.0f + __builtin_amdgcn_exp2f(-gg * L2E)));
        LAS float* gl = (LAS float*)(lds + L_G);
        gl[lane] = sg;
        gl[16 + lane] = bf2f(x[b * D + col]);
      }
    }
    STP(5);
  } else {
    const unsigned E = __hip_atomic_load((GAS unsigned*)(wsg + WS_EPOCH), RLX_AGENT);
    const int rs = lane / 7, cc = lane - rs * 7;
    const __amdgpu_buffer_rsrc_t r1 = __builtin_amdgcn_make_buffer_rsrc((void*)w1, 0, D * F * 2, 0x00020000);
    const __amdgpu_buffer_rsrc_t r2 = __builtin_amdgcn_make_buffer_rsrc((void*)(w2 + (size_t)j32 * 56 * D), 0, 56 * 896, 0x00020000);
    const unsigned o1 = lane < 63 ? (unsigned)(rs * (F * 2) + j32 * 112 + cc * 16) : OOB;
    v4u wv[10];
#pragma unroll
    for (int k = 0; k < 10; ++k) wv[k] = __builtin_amdgcn_raw_buffer_load_b128(r1, o1, (unsigned)(k * 9 * F * 2), AUXPOL);
    const float b1v = bf2f(b1[j32 * 56 + (lane < 56 ? lane : 55)]);
    tag = E * 0x9E3779B1u + 0x7F4A7C15u;
    BAR();
    BAR();
    const LAS u16* xn = (const LAS u16*)(lds + L_XN + b * 896);
    float acc[8] = {0, 0, 0, 0, 0, 0, 0, 0};
    const int rsc = rs < 9 ? rs : 8;
#pragma unroll 1
    for (int k0 = 0; k0 < 50; k0 += 10) {
#pragma unroll
      for (int k = 0; k < 10; ++k) {
        const int row = (k0 + k) * 9 + rsc;
        const float xv = bf2f(xn[row < D ? row : D - 1]);
#pragma unroll
        for (int e = 0; e < 4; ++e) { acc[2 * e] += xv * bflo(wv[k][e]); acc[2 * e + 1] += xv * bfhi(wv[k][e]); }
      }
      SB();
#pragma unroll
      for (int k = 0; k < 10; ++k) wv[k] = __builtin_amdgcn_raw_buffer_load_b128(r1, o1, (unsigned)((k0 + 10 + k) * 9 * F * 2), AUXPOL);
      SB();
    }
    const unsigned o2 = lane < 56 ? (unsigned)(lane * 16) : OOB;
    v4u w2v[14];
#pragma unroll
    for (int k = 0; k < 14; ++k) w2v[k] = __builtin_amdgcn_raw_buffer_load_b128(r2, o2, (unsigned)(k * 896), AUXPOL);
    LAS float* hred = (LAS float*)(lds + L_HRED);
    if (lane < 63) { *(LAS f32x4*)(hred + rs * 56 + cc * 8) = f32x4{acc[0], acc[1], acc[2], acc[3]}; *(LAS f32x4*)(hred + rs * 56 + cc * 8 + 4) = f32x4{acc[4], acc[5], acc[6], acc[7]}; }
    WAITL();
    LAS float* hs = (LAS float*)(lds + L_HS);
    if (lane < 56) {
      float s = 0.f;
#pragma unroll
      for (int r = 0; r < 9; ++r) s += hred[r * 56 + lane];
      const float t = rbf(rbf(s) + b1v);
      const float u = 0.7978845608028654f * (t + 0.044715f * t * t * t);
      const float e2 = __builtin_amdgcn_exp2f(u * (2.0f * L2E));
      const float th = 1.0f - 2.0f / (e2 + 1.0f);
      hs[lane] = rbf(0.5f * t * (1.0f + th));
    }
    WAITL();
    float a2[8] = {0, 0, 0, 0, 0, 0, 0, 0};
#pragma unroll 1
    for (int k0 = 0; k0 < 56; k0 += 14) {
#pragma unroll
      for (int k = 0; k < 14; ++k) {
        const float hv = hs[k0 + k];
#pragma unroll
        for (int e = 0; e < 4; ++e) { a2[2 * e] += hv * bflo(w2v[k][e]); a2[2 * e + 1] += hv * bfhi(w2v[k][e]); }
      }
      SB();
#pragma unroll
      for (int k = 0; k < 14; ++k) w2v[k] = __builtin_amdgcn_raw_buffer_load_b128(r2, o2, (unsigned)((k0 + 14 + k) * 896), AUXPOL);
      SB();
    }
    if (lane < 56) {
#pragma unroll
      for (int e = 0; e < 8; ++e) {
        const int col = lane * 8 + e, dst = col / 14, ci = col - dst * 14;
        st_gran((GAS u64*)(wsg + WS_FH + (((b * 32 + dst) * 32 + j32) * 16 + ci) * 8), tag, __float_as_uint(a2[e]));
      }
    }
    STP(3);
    {
      const u16* src = wo + ((size_t)(kvh * 512 + sp * 128)) * D + lane * 8;
#pragma unroll 1
      for (int it = 0; it < 112; it += 28) {
#pragma unroll
        for (int k = 0; k < 28; ++k)
          __builtin_amdgcn_global_load_lds((const unsigned*)(src + (size_t)(it + k) * 512), (unsigned*)(lds_ + L_WO + (it + k) * 1024), 16, 0, AUXPOL);
        WAITV(0);
      }
    }
    STP(4);
  }
  BAR();
  const int pair = b * 8 + kvh;
  if (w < 8) {
    const int h = tid >> 8;
    const LAS float* ml = (const LAS float*)(lds + L_ML);
    float M = -INFINITY;
#pragma unroll
    for (int ww = 0; ww < 8; ++ww) M = fmaxf(M, ml[ww * 4 + h * 2]);
    float L = 0.f, o = 0.f;
    const LAS float* ac = (const LAS float*)(lds + L_SC) + tid;
#pragma unroll
    for (int ww = 0; ww < 8; ++ww) { const float sc = __builtin_amdgcn_exp2f((ml[ww * 4 + h * 2] - M) * L2E); L += sc * ml[ww * 4 + h * 2 + 1]; o += sc * ac[ww * SC_STRIDE]; }
    const int dst = tid >> 7, idx = tid & 127;
    if (dst != sp) {
      GAS u64* pb = (GAS u64*)(wsg + WS_PBOX + ((pair * 4 + dst) * 4 + sp) * PMSG);
      st_gran(pb + idx, tag, __float_as_uint(o));
      if (idx == 0) { st_gran(pb + 128, tag, __float_as_uint(M)); st_gran(pb + 129, tag, __float_as_uint(L)); }
    } else {
      GAS u64* pb = (GAS u64*)(wsg + WS_PBOX + ((pair * 4 + sp) * 4) * PMSG);
      float ov[3], mv[3], lv[3];
      for (;;) {
        asm volatile("" ::: "memory");
        u64 gv[3], gm[3];
#pragma unroll
        for (int k = 0; k < 3; ++k) {
          const int src = (sp + 1 + k) & 3;
          gv[k] = ld_gran(pb + src * (PMSG / 8) + idx);
          gm[k] = ld_gran(pb + src * (PMSG / 8) + 128 + (lane & 1));
        }
        bool ok = true;
#pragma unroll
        for (int k = 0; k < 3; ++k) {
          ok &= ((unsigned)(gv[k] >> 32) == tag) && ((unsigned)(gm[k] >> 32) == tag);
          ov[k] = __uint_as_float((unsigned)gv[k]);
          mv[k] = __builtin_bit_cast(float, __builtin_amdgcn_readlane((int)(unsigned)gm[k], 0));
          lv[k] = __builtin_bit_cast(float, __builtin_amdgcn_readlane((int)(unsigned)gm[k], 1));
        }
        if (__all(ok)) break;
        if (rt() - tstart > TMO) break;
      }
      const float Mx = fmaxf(fmaxf(M, mv[0]), fmaxf(mv[1], mv[2]));
      const float eo = __builtin_amdgcn_exp2f((M - Mx) * L2E), e0 = __builtin_amdgcn_exp2f((mv[0] - Mx) * L2E), e1 = __builtin_amdgcn_exp2f((mv[1] - Mx) * L2E), e2 = __builtin_amdgcn_exp2f((mv[2] - Mx) * L2E);
      const float Lt = eo * L + e0 * lv[0] + e1 * lv[1] + e2 * lv[2];
      const float of = (eo * o + e0 * ov[0] + e1 * ov[1] + e2 * ov[2]) / Lt;
      ((LAS float*)(lds + L_OS))[idx] = rbf(of);
    }
  }
  STP(w < 8 ? 5 : 6);
  BAR();
  if (w < 8) {
    const int rg = lane & 7, cc = (lane >> 3) < 7 ? (lane >> 3) : 6;
    float a[8] = {0, 0, 0, 0, 0, 0, 0, 0};
    const LAS float* os = (const LAS float*)(lds + L_OS) + rg;
    const LAS unsigned char* wp = lds + L_WO + rg * 896 + (7 * w + cc) * 16;
#pragma unroll
    for (int r = 0; r < 16; ++r) {
      const v4u wv = *(const LAS v4u*)(wp + r * 8 * 896);
      const float ov = os[8 * r];
#pragma unroll
      for (int e = 0; e < 4; ++e) { a[2 * e] += ov * bflo(wv[e]); a[2 * e + 1] += ov * bfhi(wv[e]); }
    }
    float y[4];
#pragma unroll
    for (int v = 0; v < 4; ++v) { const float t1 = a[v] + dpp<HMIR>(a[v]), t2 = a[v + 4] + dpp<HMIR>(a[v + 4]); y[v] = (lane & 4) ? t2 : t1; }
    float z[2];
#pragma unroll
    for (int v = 0; v < 2; ++v) { const float t1 = y[v] + dpp<XOR2>(y[v]), t2 = y[v + 2] + dpp<XOR2>(y[v + 2]); z[v] = (lane & 2) ? t2 : t1; }
    const float t1 = z[0] + dpp<XOR1>(z[0]), t2 = z[1] + dpp<XOR1>(z[1]);
    const float s = (lane & 1) ? t2 : t1;
    if (lane < 56) {
      const int col = 56 * w + lane;
      const int dst = col / 14, ci = col - dst * 14;
      st_gran((GAS u64*)(wsg + WS_FS + (((b * 32 + dst) * 32 + j32) * 16 + ci) * 8), tag, __float_as_uint(s));
    }
  }
  if (w == 0) {
    GAS u64* fb = (GAS u64*)(wsg + WS_FS + (b * 32 + j32) * 4096) + lane;
    float s;
    for (;;) {
      asm volatile("" ::: "memory");
      u64 gv[8]; bool ok = true; s = 0.f;
#pragma unroll
      for (int k = 0; k < 8; ++k) gv[k] = ld_gran(fb + k * 64);
#pragma unroll
      for (int k = 0; k < 8; ++k) { ok &= ((unsigned)(gv[k] >> 32) == tag) || ((lane & 15) >= 14); s += __uint_as_float((unsigned)gv[k]); }
      if (__all(ok)) break;
      if (rt() - tstart > TMO) break;
    }
    s = sum16(s); s = sum32(s);
    if (lane < 14) {
      const int col = j32 * 14 + lane;
      const float sg = ((const LAS float*)(lds + L_G))[lane], xv = ((const LAS float*)(lds + L_G))[16 + lane];
      const float r = xv + rbf(rbf(s) * sg);
      out[b * D + col] = (u16)(pk2(r, r) & 0xffffu);
    }
    if (bid == 0 && lane == 0) {
      const unsigned E = __hip_atomic_load((GAS unsigned*)(wsg + WS_EPOCH), RLX_AGENT);
      __hip_atomic_store((GAS unsigned*)(wsg + WS_EPOCH), E + 1u, RLX_AGENT);
    }
  }
  STP(7);
}

extern "C" void kernel_launch(void* const* d_in, const int* in_sizes, int n_in, void* d_out, int out_size, void* d_ws, size_t ws_size, hipStream_t stream) {
  static bool attr_set = false;
  if (!attr_set) { (void)hipFuncSetAttribute((const void*)fused, hipFuncAttributeMaxDynamicSharedMemorySize, L_END); attr_set = true; }
  hipLaunchKernelGGL(fused, dim3(256), dim3(NTHREADS), L_END, stream,
                     (const u16*)d_in[0], (const u16*)d_in[1], (const u16*)d_in[2], (const u16*)d_in[3], (const u16*)d_in[4],
                     (const u16*)d_in[6], (const u16*)d_in[7], (const u16*)d_in[8], (const u16*)d_in[9], (u16*)d_out, (unsigned char*)d_ws);
}
```

```cpp
#define VPLAIN_FROM 8
#include <hip/hip_runtime.h>
#include <hip/hip_bf16.h>
#include <stdint.h>

typedef unsigned short u16;
typedef unsigned v4u __attribute__((ext_vector_type(4)));
typedef unsigned v2u __attribute__((ext_vector_type(2)));
typedef float f32x4 __attribute__((ext_vector_type(4)));
typedef short bf16x8 __attribute__((ext_vector_type(8)));
typedef __bf16 bf16x2 __attribute__((ext_vector_type(2)));
typedef unsigned long long u64;
#define LAS __attribute__((address_space(3)))
#define GAS __attribute__((address_space(1)))
#define RLX_AGENT __ATOMIC_RELAXED, __HIP_MEMORY_SCOPE_AGENT

constexpr int D = 448, NQ = 4096, SK = 8192, F = 1792;
#define ABL_W1 0
#define ABL_W2 0
#define ABL_WO 0
#define WDELAY 6
#define POLLSLEEP 16
#define AUXPOL 2
constexpr int NBATCH = 32;
constexpr float L2E = 1.4426950408889634f;
constexpr int NTHREADS = 704;

constexpr unsigned WS_EPOCH = 0;
constexpr unsigned WS_QBOX = 4096;
constexpr unsigned WS_PBOX = WS_QBOX + 8 * 8 * 32 * 64;
constexpr unsigned PMSG = 136 * 8;
constexpr unsigned WS_FS = WS_PBOX + 64 * 16 * PMSG;
constexpr unsigned WS_FH = WS_FS + 8 * 32 * 32 * 128;
constexpr unsigned WS_END = WS_FH + 8 * 32 * 32 * 128;

constexpr int L_WO = 0;
constexpr int SC_STRIDE = 8 * 68;
constexpr int L_SC = L_WO + 128 * 896;
constexpr int L_XN = L_SC + 8 * SC_STRIDE * 4;
constexpr int L_WQ = L_XN + 8 * 896;
constexpr int L_QV = L_WQ + 448 * 32;
constexpr int L_TAG = L_QV + 1024, L_QS = L_TAG + 16, L_OS = L_QS + 256, L_G = L_OS + 512, L_HRED = L_G + 128, L_ML = L_HRED + 8 * 56 * 4, L_CNT = L_ML + 128;
constexpr int L_TS = L_CNT + 16;
constexpr int L_END = L_TS;
constexpr int L_MPART = L_SC;
static_assert(L_END <= 160 * 1024, "lds");

__device__ __forceinline__ float bf2f(u16 b) { return __uint_as_float(((unsigned)b) << 16); }
__device__ __forceinline__ float bflo(unsigned w) { return __uint_as_float(w << 16); }
__device__ __forceinline__ float bfhi(unsigned w) { return __uint_as_float(w & 0xffff0000u); }
__device__ __forceinline__ unsigned pk2(float a, float b) { bf16x2 v = {(__bf16)a, (__bf16)b}; return __builtin_bit_cast(unsigned, v); }
__device__ __forceinline__ float rbf(float f) { return bflo(pk2(f, f)); }
__device__ __forceinline__ float dot2(unsigned k, unsigned q, float acc) {
  return __builtin_amdgcn_fdot2_f32_bf16(__builtin_bit_cast(bf16x2, k), __builtin_bit_cast(bf16x2, q), acc, false);
}
template <int CTRL> __device__ __forceinline__ float dpp(float x) {
  return __builtin_bit_cast(float, __builtin_amdgcn_mov_dpp(__builtin_bit_cast(int, x), CTRL, 0xf, 0xf, true));
}
constexpr int XOR1 = 0xB1, XOR2 = 0x4E, HMIR = 0x141, ROR8 = 0x128;
__device__ __forceinline__ float sum16(float x) {
  auto s = __builtin_amdgcn_permlane16_swap(__float_as_uint(x), __float_as_uint(x), false, false); return __uint_as_float(s[0]) + __uint_as_float(s[1]); }
__device__ __forceinline__ float sum32(float x) {
  auto s = __builtin_amdgcn_permlane32_swap(__float_as_uint(x), __float_as_uint(x), false, false); return __uint_as_float(s[0]) + __uint_as_float(s[1]); }
__device__ __forceinline__ float max16(float x) {
  auto s = __builtin_amdgcn_permlane16_swap(__float_as_uint(x), __float_as_uint(x), false, false); return fmaxf(__uint_as_float(s[0]), __uint_as_float(s[1])); }
__device__ __forceinline__ float max32(float x) {
  auto s = __builtin_amdgcn_permlane32_swap(__float_as_uint(x), __float_as_uint(x), false, false); return fmaxf(__uint_as_float(s[0]), __uint_as_float(s[1])); }
__device__ __forceinline__ float wsum64(float x) {
  x += dpp<XOR1>(x); x += dpp<XOR2>(x); x += dpp<HMIR>(x); x += dpp<ROR8>(x); x = sum16(x); return sum32(x); }
__device__ __forceinline__ f32x4 mfma(v4u a, v4u b, f32x4 c) {
  return __builtin_amdgcn_mfma_f32_16x16x32_bf16(__builtin_bit_cast(bf16x8, a), __builtin_bit_cast(bf16x8, b), c, 0, 0, 0);
}
__device__ __forceinline__ u64 rt() { return __builtin_amdgcn_s_memrealtime(); }
#define WAITV(n) asm volatile("s_waitcnt vmcnt(" #n ")" ::: "memory")
#define WAITL() asm volatile("s_waitcnt lgkmcnt(0)" ::: "memory")
#define SB() __builtin_amdgcn_sched_barrier(0)
#define BAR() do { WAITL(); __builtin_amdgcn_s_barrier(); asm volatile("" ::: "memory"); } while (0)
__device__ __forceinline__ void st_gran(GAS u64* p, unsigned tag, unsigned val) { __hip_atomic_store(p, ((u64)tag << 32) | val, RLX_AGENT); }
__device__ __forceinline__ u64 ld_gran(GAS u64* p) { return __hip_atomic_load(p, RLX_AGENT); }
constexpr u64 TMO = 400000;
constexpr unsigned OOB = 0x7fffff00u;

#define STP(k) do {} while (0)

__global__ __launch_bounds__(NTHREADS) void fused(const u16* __restrict__ x, const u16* __restrict__ kc, const u16* __restrict__ vc, const u16* __restrict__ wq, const u16* __restrict__ wo,
                                             const u16* __restrict__ w1, const u16* __restrict__ b1, const u16* __restrict__ w2, const u16* __restrict__ b2, u16* __restrict__ out, unsigned char* __restrict__ ws) {
  extern __shared__ __attribute__((aligned(16))) unsigned char lds_[];
  LAS unsigned char* lds = (LAS unsigned char*)lds_;
  const int bid = blockIdx.x; const int b = bid >> 5, j32 = bid & 31, kvh = j32 >> 2, sp = bid & 3;
  const int tid = threadIdx.x, lane = tid & 63; const int w = __builtin_amdgcn_readfirstlane(tid >> 6);
  GAS unsigned char* wsg = (GAS unsigned char*)ws;
  STP(0);
  const u64 tstart = rt();
  unsigned tag = 0;
  const size_t kvbase = ((size_t)(b * 8 + kvh) * SK + (size_t)sp * 2048) * 512;
  float o_own = 0.f, M_own = 0.f, L_own = 0.f;

  if (w < 8) {
    const __amdgpu_buffer_rsrc_t rk = __builtin_amdgcn_make_buffer_rsrc((void*)((const char*)kc + kvbase), 0, 1u << 20, 0x00020000);
    const __amdgpu_buffer_rsrc_t rv = __builtin_amdgcn_make_buffer_rsrc((void*)((const char*)vc + kvbase), 0, 1u << 20, 0x00020000);
    const __amdgpu_buffer_rsrc_t rx = __builtin_amdgcn_make_buffer_rsrc((void*)x, 0, 8 * 896, 0x00020000);
    const unsigned koff = lane * 16;
    const unsigned voff = (lane >> 4) * 512 + (lane & 15) * 16;
    auto ldk = [&](int i, v4u (&r)[4]) {
#pragma unroll
      for (int u = 0; u < 4; ++u) r[u] = __builtin_amdgcn_raw_buffer_load_b128(rk, koff + u * 1024, (unsigned)(i * 8 + w) * 4096u, 2);
    };
    auto ldv = [&](int i, v4u (&r)[4]) {
      if (b >= VPLAIN_FROM) {
#pragma unroll
        for (int u = 0; u < 4; ++u) r[u] = __builtin_amdgcn_raw_buffer_load_b128(rv, voff + (u >> 1) * 2048 + (u & 1) * 256, (unsigned)(i * 8 + w) * 4096u, 0);
      } else {
#pragma unroll
        for (int u = 0; u < 4; ++u) r[u] = __builtin_amdgcn_raw_buffer_load_b128(rv, voff + (u >> 1) * 2048 + (u & 1) * 256, (unsigned)(i * 8 + w) * 4096u, 2);
      }
    };
    v4u K0[4], K1[4], K2[4], K3[4], K4[4], K5[4], K6[4], K7[4];
    const v4u xr = __builtin_amdgcn_raw_buffer_load_b128(rx, lane < 56 ? (unsigned)(w * 896 + lane * 16) : OOB, 0, 0);
    SB();
    ldk(0, K0); SB();
    __builtin_amdgcn_s_sleep(WDELAY);
    const int mrs = lane / 7, mcc = lane - mrs * 7;
    const __amdgpu_buffer_rsrc_t r1 = __builtin_amdgcn_make_buffer_rsrc((void*)w1, 0, D * F * 2, 0x00020000);
    const __amdgpu_buffer_rsrc_t r2 = __builtin_amdgcn_make_buffer_rsrc((void*)(w2 + (size_t)j32 * 56 * D), 0, 56 * 896, 0x00020000);
    v4u W1[7], W2[7];
    {
      const unsigned o1 = lane < 56 ? (unsigned)((56 * w + mrs) * (F * 2) + j32 * 112 + mcc * 16) : OOB;
#pragma unroll
      for (int k = 0; k < 7; ++k) W1[k] = __builtin_amdgcn_raw_buffer_load_b128(r1, ABL_W1 ? OOB : o1, (unsigned)(k * 8 * F * 2), AUXPOL);
      const unsigned o2 = lane < 56 ? (unsigned)(7 * w * 896 + lane * 16) : OOB;
#pragma unroll
      for (int k = 0; k < 7; ++k) W2[k] = __builtin_amdgcn_raw_buffer_load_b128(r2, ABL_W2 ? OOB : o2, (unsigned)(k * 896), AUXPOL);
    }
    const float b1v = bf2f(b1[j32 * 56 + 7 * w + (lane < 7 ? lane : 6)]);
    SB();
    {
      float v[8];
#pragma unroll
      for (int k = 0; k < 4; ++k) { v[2 * k] = bflo(xr[k]); v[2 * k + 1] = bfhi(xr[k]); }
      float s = ((v[0] + v[1]) + (v[2] + v[3])) + ((v[4] + v[5]) + (v[6] + v[7]));
      s = wsum64(s);
      const float mean = s * (1.0f / 448.0f);
      float q = 0.f;
#pragma unroll
      for (int k = 0; k < 8; ++k) { const float d = v[k] - mean; q += d * d; }
      q = lane < 56 ? q : 0.f;
      q = wsum64(q);
      const float meanb = rbf(mean);
      const float den = rbf(sqrtf(rbf(rbf(q * (1.0f / 448.0f)) + 1e-5f)));
      float rc = __builtin_amdgcn_rcpf(den); rc = rc * (2.0f - den * rc);
      v4u o;
#pragma unroll
      for (int k = 0; k < 4; ++k) { const unsigned d2 = pk2(v[2 * k] - meanb, v[2 * k + 1] - meanb); o[k] = pk2(bflo(d2) * rc, bfhi(d2) * rc); }
      if (lane < 56) *(LAS v4u*)(lds + L_XN + w * 896 + lane * 16) = o;
    }
    STP(8);
    BAR();
    tag = *(const LAS unsigned*)(lds + L_TAG);
    volatile LAS unsigned* cnt = (volatile LAS unsigned*)(lds + L_CNT);
    WAITV(15); SB(); STP(9); ldk(1, K1); SB();
    WAITV(12); SB(); STP(11); ldk(2, K2); SB();
    {
      const int rsc = mrs < 8 ? mrs : 7;
      const LAS u16* xn = (const LAS u16*)(lds + L_XN + b * 896) + 56 * w + rsc;
      float acc[8] = {0, 0, 0, 0, 0, 0, 0, 0};
#pragma unroll
      for (int k = 0; k < 7; ++k) {
        const float xv = bf2f(xn[8 * k]);
#pragma unroll
        for (int e = 0; e < 4; ++e) { acc[2 * e] += xv * bflo(W1[k][e]); acc[2 * e + 1] += xv * bfhi(W1[k][e]); }
      }
      LAS float* scr = (LAS float*)(lds + L_SC) + w * SC_STRIDE;
      if (lane < 56) { *(LAS f32x4*)(scr + mrs * 56 + mcc * 8) = f32x4{acc[0], acc[1], acc[2], acc[3]}; *(LAS f32x4*)(scr + mrs * 56 + mcc * 8 + 4) = f32x4{acc[4], acc[5], acc[6], acc[7]}; }
      WAITL();
      const int lc = lane < 56 ? lane : 55;
      float s = 0.f;
#pragma unroll
      for (int r = 0; r < 8; ++r) s += scr[r * 56 + lc];
      if (lane < 56) ((LAS float*)(lds + L_HRED))[w * 56 + lane] = s;
      WAITL();
      if (lane == 0) __hip_atomic_fetch_add((LAS unsigned*)(lds + L_CNT), 1u, __ATOMIC_RELAXED, __HIP_MEMORY_SCOPE_WORKGROUP);
    }
    STP(12);
    WAITV(0); SB(); STP(13); ldk(3, K3); SB();
    WAITV(0); SB(); STP(14); ldk(4, K4); SB();
    {
      while (cnt[0] < 8u) __builtin_amdgcn_s_sleep(1);
      asm volatile("" ::: "memory");
      float hs = 0.f;
      {
        const LAS float* hr = (const LAS float*)(lds + L_HRED) + 7 * w + (lane < 7 ? lane : 6);
#pragma unroll
        for (int ww = 0; ww < 8; ++ww) hs += hr[ww * 56];
      }
      const float t = rbf(rbf(hs) + b1v);
      const float u = 0.7978845608028654f * (t + 0.044715f * t * t * t);
      const float e2 = __builtin_amdgcn_exp2f(u * (2.0f * L2E));
      const float th = 1.0f - 2.0f / (e2 + 1.0f);
      const float hg = rbf(0.5f * t * (1.0f + th));
      float a2[8] = {0, 0, 0, 0, 0, 0, 0, 0};
#pragma unroll
      for (int k = 0; k < 7; ++k) {
        const float hv = __builtin_bit_cast(float, __builtin_amdgcn_readlane(__builtin_bit_cast(int, hg), k));
#pragma unroll
        for (int e = 0; e < 4; ++e) { a2[2 * e] += hv * bflo(W2[k][e]); a2[2 * e + 1] += hv * bfhi(W2[k][e]); }
      }
      LAS float* mp = (LAS float*)(lds + L_MPART) + w * 448 + lane * 8;
      if (lane < 56) { *(LAS f32x4*)(mp) = f32x4{a2[0], a2[1], a2[2], a2[3]}; *(LAS f32x4*)(mp + 4) = f32x4{a2[4], a2[5], a2[6], a2[7]}; }
      WAITL();
      if (lane == 0) __hip_atomic_fetch_add((LAS unsigned*)(lds + L_CNT + 4), 1u, __ATOMIC_RELAXED, __HIP_MEMORY_SCOPE_WORKGROUP);
    }
    WAITV(0); SB(); ldk(5, K5); SB();
    WAITV(0); SB(); STP(16); ldk(6, K6); SB();
    {
      while (cnt[1] < 8u) __builtin_amdgcn_s_sleep(1);
      asm volatile("" ::: "memory");
      if (lane < 56) {
        const int col = 56 * w + lane;
        const LAS float* mp = (const LAS float*)(lds + L_MPART) + col;
        float s = 0.f;
#pragma unroll
        for (int ww = 0; ww < 8; ++ww) s += mp[ww * 448];
        const int dst = col / 14, ci = col - dst * 14;
        st_gran((GAS u64*)(wsg + WS_FH + (((b * 32 + dst) * 32 + j32) * 16 + ci) * 8), tag, __float_as_uint(s));
      }
    }
    STP(15);
    WAITV(0); SB(); ldk(7, K7); SB();
    STP(1);
    BAR();
    STP(2);
    const int c = lane & 31;
    const v4u q0 = *(const LAS v4u*)(lds + L_QV + c * 16), q1 = *(const LAS v4u*)(lds + L_QV + 512 + c * 16);
    LAS float* scw = (LAS float*)(lds + L_SC) + w * SC_STRIDE;
    float mrun = -INFINITY;
    const int hsel = (lane >> 2) & 1, ksel = 2 * ((lane >> 3) & 3) + (lane >> 5);
    auto kcomp = [&](int i, const v4u (&r)[4]) {
      float xx[8];
#pragma unroll
      for (int u = 0; u < 4; ++u) {
        float a = 0.f, bb = 0.f;
#pragma unroll
        for (int k = 0; k < 4; ++k) { a = dot2(r[u][k], q0[k], a); bb = dot2(r[u][k], q1[k], bb); }
        xx[2 * u] = a; xx[2 * u + 1] = bb;
      }
      float y[4];
#pragma unroll
      for (int v = 0; v < 4; ++v) {
        auto s = __builtin_amdgcn_permlane16_swap(__float_as_uint(xx[v]), __float_as_uint(xx[v + 4]), false, false);
        y[v] = __uint_as_float(s[0]) + __uint_as_float(s[1]);
      }
      float z[2];
#pragma unroll
      for (int v = 0; v < 2; ++v) {
        const float t1 = y[v] + dpp<ROR8>(y[v]), t2 = y[v + 2] + dpp<ROR8>(y[v + 2]);
        z[v] = (lane & 8) ? t2 : t1;
      }
      const float t1 = z[0] + dpp<HMIR>(z[0]), t2 = z[1] + dpp<HMIR>(z[1]);
      float s = (lane & 4) ? t2 : t1;
      s += dpp<XOR2>(s); s += dpp<XOR1>(s);
      s = rbf(s) * 0.0625f;
      mrun = fmaxf(mrun, s);
      if ((lane & 3) == 0) scw[hsel * 256 + i * 8 + ksel] = s;
    };
    kcomp(0, K0); SB(); kcomp(1, K1); SB(); kcomp(2, K2); SB(); kcomp(3, K3); SB(); kcomp(4, K4); SB(); kcomp(5, K5); SB(); kcomp(6, K6); SB();
#pragma unroll 1
    for (int i = 7; i < NBATCH - 1; i += 2) {
      WAITV(0); SB();
      ldk(i + 1, K0); SB();
      kcomp(i, K7); SB();
      WAITV(0); SB();
      ldk(i + 2, K7); SB();
      kcomp(i + 1, K0); SB();
    }
    WAITV(0); SB();
    ldv(0, K0); SB();
    kcomp(NBATCH - 1, K7); SB();
    STP(3);
    mrun = fmaxf(mrun, dpp<ROR8>(mrun)); mrun = max16(mrun); mrun = max32(mrun);
    const float m0 = __builtin_bit_cast(float, __builtin_amdgcn_readlane(__builtin_bit_cast(int, mrun), 0));
    const float m1 = __builtin_bit_cast(float, __builtin_amdgcn_readlane(__builtin_bit_cast(int, mrun), 4));
    const int hh = lane >> 5;
    const float mh = hh ? m1 : m0;
    float lsum;
    {
      WAITL();
      const f32x4 s0 = *(LAS f32x4*)(scw + hh * 256 + c * 8), s1 = *(LAS f32x4*)(scw + hh * 256 + c * 8 + 4);
      WAITL();
      unsigned pw[8]; lsum = 0.f;
#pragma unroll
      for (int e = 0; e < 4; ++e) {
        const float p0 = __builtin_amdgcn_exp2f((s0[e] - mh) * L2E), p1 = __builtin_amdgcn_exp2f((s1[e] - mh) * L2E);
        lsum += p0 + p1;
        pw[e] = pk2(p0, p0); pw[e + 4] = pk2(p1, p1);
      }
      LAS unsigned* pwl = (LAS unsigned*)scw;
#pragma unroll
      for (int g = 0; g < 4; ++g) *(LAS v2u*)(pwl + (hh * 4 + g) * 68 + 2 * c) = v2u{pw[g], pw[g + 4]};
      lsum += dpp<XOR1>(lsum); lsum += dpp<XOR2>(lsum); lsum += dpp<HMIR>(lsum); lsum += dpp<ROR8>(lsum); lsum = sum16(lsum);
      WAITL();
    }
    f32x4 acc0 = {0, 0, 0, 0}, acc1 = {0, 0, 0, 0};
    const int ii = lane & 15, g = lane >> 4, hV = ii >> 3, jj = ii & 7;
    unsigned msk[4];
#pragma unroll
    for (int r = 0; r < 4; ++r) msk[r] = (r == (jj >> 1)) ? ((jj & 1) ? 0xffff0000u : 0x0000ffffu) : 0u;
    const LAS unsigned* prd = (const LAS unsigned*)scw + (hV * 4 + g) * 68;
    auto vcomp = [&](int i, const v4u (&r)[4]) {
      const v2u pw = *(const LAS v2u*)(prd + 2 * i);
#pragma unroll
      for (int rg = 0; rg < 2; ++rg) {
        const unsigned pv = pw[rg];
        const v4u a = {pv & msk[0], pv & msk[1], pv & msk[2], pv & msk[3]};
        acc0 = mfma(a, r[2 * rg], acc0);
        acc1 = mfma(a, r[2 * rg + 1], acc1);
      }
    };
#pragma unroll 1
    for (int i = 0; i < NBATCH; i += 2) {
      WAITV(0); SB();
      ldv(i + 1, K7); SB();
      vcomp(i, K0); SB();
      WAITV(0); SB();
      if (i + 2 < NBATCH) ldv(i + 2, K0);
      SB();
      vcomp(i + 1, K7); SB();
    }
    STP(4);
    LAS float* accw = (LAS float*)(lds + L_SC) + w * SC_STRIDE + hh * 256 + 8 * (lane & 15) + 4 * ((lane >> 4) & 1);
    *(LAS f32x4*)(accw) = acc0; *(LAS f32x4*)(accw + 128) = acc1;
    LAS float* ml = (LAS float*)(lds + L_ML) + w * 4;
    if (c == 0) { ml[hh * 2] = mh; ml[hh * 2 + 1] = lsum; }
  } else if (w == 8) {
    const unsigned E = __hip_atomic_load((GAS unsigned*)(wsg + WS_EPOCH), RLX_AGENT);
    const int pidx = sp * 8 + b;
    {
      const u16* src = wq + (size_t)(lane >> 1) * NQ + kvh * 512 + pidx * 16 + (lane & 1) * 8;
#pragma unroll
      for (int it = 0; it < 14; ++it)
        __builtin_amdgcn_global_load_lds((const unsigned*)(src + (size_t)it * 32 * NQ), (unsigned*)(lds_ + L_WQ + it * 1024), 16, 0, AUXPOL);
    }
    unsigned warm;
    {
      const GAS unsigned char* p;
      if (lane < 16) p = (const GAS unsigned char*)vc + kvbase + (size_t)lane * 65536;
      else if (lane < 32) p = (const GAS unsigned char*)kc + kvbase + (size_t)(lane - 16) * 65536;
      else if (lane < 40) p = wsg + WS_PBOX + (size_t)(b * 8 + kvh) * 16 * PMSG + (lane - 32) * 2176;
      else if (lane < 48) p = wsg + WS_FS + (size_t)b * 131072 + (lane - 40) * 16384;
      else if (lane < 56) p = wsg + WS_FH + (size_t)b * 131072 + (lane - 48) * 16384;
      else p = (const GAS unsigned char*)wo + ((size_t)(kvh * 512 + sp * 128)) * 896 + (lane - 56) * 14336;
      warm = __builtin_nontemporal_load((const GAS unsigned*)p);
    }
    tag = E * 0x9E3779B1u + 0x7F4A7C15u;
    if (lane < 4) ((LAS unsigned*)(lds + L_CNT))[lane] = 0u;
    if (lane == 0) *(LAS unsigned*)(lds + L_TAG) = tag;
    BAR();
    WAITV(1);
    STP(10);
    {
      const int i16 = lane & 15, g = lane >> 4;
      const unsigned aaddr = L_XN + (i16 & 7) * 896 + g * 16;
      const unsigned baddr = (unsigned)(size_t)(lds) + L_WQ + (8 * g + (i16 >> 2)) * 32 + (i16 & 3) * 8;
      v4u a[14]; v2u b0[14], b1v[14];
#pragma unroll
      for (int ks = 0; ks < 14; ++ks) a[ks] = *(const LAS v4u*)(lds + aaddr + ks * 64);
#pragma unroll
      for (int ks = 0; ks < 14; ++ks)
        asm volatile("ds_read_b64_tr_b16 %0, %2 offset:%3\n\tds_read_b64_tr_b16 %1, %2 offset:%4" : "=&v"(b0[ks]), "=&v"(b1v[ks]) : "v"(baddr), "i"(ks * 1024), "i"(ks * 1024 + 128) : "memory");
      asm volatile("s_waitcnt lgkmcnt(0)" : "+v"(b0[0]), "+v"(b0[1]), "+v"(b0[2]), "+v"(b0[3]), "+v"(b0[4]), "+v"(b0[5]), "+v"(b0[6]), "+v"(b0[7]), "+v"(b0[8]), "+v"(b0[9]), "+v"(b0[10]), "+v"(b0[11]), "+v"(b0[12]), "+v"(b0[13]) :: "memory");
      asm volatile("" : "+v"(b1v[0]), "+v"(b1v[1]), "+v"(b1v[2]), "+v"(b1v[3]), "+v"(b1v[4]), "+v"(b1v[5]), "+v"(b1v[6]), "+v"(b1v[7]), "+v"(b1v[8]), "+v"(b1v[9]), "+v"(b1v[10]), "+v"(b1v[11]), "+v"(b1v[12]), "+v"(b1v[13]) :: "memory");
      SB();
      f32x4 dq0 = {0, 0, 0, 0}, dq1 = {0, 0, 0, 0};
#pragma unroll
      for (int ks = 0; ks < 14; ks += 2) {
        dq0 = mfma(a[ks], v4u{b0[ks][0], b0[ks][1], b1v[ks][0], b1v[ks][1]}, dq0);
        dq1 = mfma(a[ks + 1], v4u{b0[ks + 1][0], b0[ks + 1][1], b1v[ks + 1][0], b1v[ks + 1][1]}, dq1);
      }
      const f32x4 dq = dq0 + dq1;
      LAS u16* qs = (LAS u16*)(lds + L_QS);
      if (g < 2) {
#pragma unroll
        for (int r = 0; r < 4; ++r) qs[(4 * g + r) * 16 + i16] = (u16)(pk2(dq[r], dq[r]) & 0xffffu);
      }
      WAITL();
      const unsigned qw = *(const LAS unsigned*)(lds + L_QS + lane * 4);
      st_gran((GAS u64*)(wsg + WS_QBOX + (((kvh * 8 + (lane >> 3)) * 32 + pidx) * 8 + (lane & 7)) * 8), tag, qw);
      if (lane == 0) ((volatile LAS unsigned*)(lds + L_CNT))[2] = 1u;
    }
    STP(1);
    {
      const __amdgpu_buffer_rsrc_t rq = __builtin_amdgcn_make_buffer_rsrc((void*)(ws + WS_QBOX + (kvh * 8 + b) * 2048), 0, 2048, 0x00020000);
      v4u g0, g1; int npoll = 0;
      for (;;) {
        asm volatile("" ::: "memory");
        ++npoll;
        __builtin_amdgcn_s_sleep(POLLSLEEP);
        g0 = __builtin_amdgcn_raw_buffer_load_b128(rq, lane * 32, 0, 16);
        g1 = __builtin_amdgcn_raw_buffer_load_b128(rq, lane * 32 + 16, 0, 16);
        const bool ok = g0[1] == tag && g0[3] == tag && g1[1] == tag && g1[3] == tag;
        if (__all(ok)) break;
        if (rt() - tstart > TMO) break;
      }
      *(LAS v4u*)(lds + L_QV + lane * 16) = v4u{g0[0], g0[2], g1[0], g1[2]};
    }
    STP(2);
    BAR();
    asm volatile("" :: "v"(warm));
    {
      GAS u64* fb = (GAS u64*)(wsg + WS_FH + (b * 32 + j32) * 4096) + lane;
      float s = 0.f;
      for (;;) {
        __builtin_amdgcn_s_sleep(64);
        asm volatile("" ::: "memory");
        bool ok = true; s = 0.f;
        u64 gv[8];
#pragma unroll
        for (int k = 0; k < 8; ++k) gv[k] = ld_gran(fb + k * 64);
#pragma unroll
        for (int k = 0; k < 8; ++k) { ok &= ((unsigned)(gv[k] >> 32) == tag) || ((lane & 15) >= 14); s += __uint_as_float((unsigned)gv[k]); }
        if (__all(ok)) break;
        if (rt() - tstart > TMO) break;
      }
      s = sum16(s); s = sum32(s);
      if (lane < 14) {
        const int col = j32 * 14 + lane;
        const float gg = rbf(rbf(s) + bf2f(b2[col]));
        const float sg = rbf(gg / (1.0f + __builtin_amdgcn_exp2f(-gg * L2E)));
        LAS float* gl = (LAS float*)(lds + L_G);
        gl[lane] = sg;
        gl[16 + lane] = bf2f(x[b * D + col]);
      }
    }
    STP(5);
  } else {
    {
      const int half = w - 9;
      const u16* src = wo + ((size_t)(kvh * 512 + sp * 128)) * D + half * 56 * 512 + lane * 8;
      __builtin_amdgcn_s_sleep(WDELAY);
#pragma unroll
      for (int k = 0; k < (ABL_WO ? 0 : 56); ++k)
        __builtin_amdgcn_global_load_lds((const unsigned*)(src + (size_t)k * 512), (unsigned*)(lds_ + L_WO + (half * 56 + k) * 1024), 16, 0, AUXPOL);
      BAR();
      BAR();
      WAITV(0);
    }
  }
  BAR();
  const int pair = b * 8 + kvh;
  if (w < 8) {
    const int h = tid >> 8;
    const LAS float* ml = (const LAS float*)(lds + L_ML);
    float M = -INFINITY;
#pragma unroll
    for (int ww = 0; ww < 8; ++ww) M = fmaxf(M, ml[ww * 4 + h * 2]);
    float L = 0.f, o = 0.f;
    const LAS float* ac = (const LAS float*)(lds + L_SC) + tid;
#pragma unroll
    for (int ww = 0; ww < 8; ++ww) { const float sc = __builtin_amdgcn_exp2f((ml[ww * 4 + h * 2] - M) * L2E); L += sc * ml[ww * 4 + h * 2 + 1]; o += sc * ac[ww * SC_STRIDE]; }
    const int dst = tid >> 7, idx = tid & 127;
    if (dst != sp) {
      GAS u64* pb = (GAS u64*)(wsg + WS_PBOX + ((pair * 4 + dst) * 4 + sp) * PMSG);
      st_gran(pb + idx, tag, __float_as_uint(o));
      if (idx == 0) { st_gran(pb + 128, tag, __float_as_uint(M)); st_gran(pb + 129, tag, __float_as_uint(L)); }
    } else {
      GAS u64* pb = (GAS u64*)(wsg + WS_PBOX + ((pair * 4 + sp) * 4) * PMSG);
      float ov[3], mv[3], lv[3];
      for (;;) {
        asm volatile("" ::: "memory");
        u64 gv[3], gm[3];
#pragma unroll
        for (int k = 0; k < 3; ++k) {
          const int src = (sp + 1 + k) & 3;
          gv[k] = ld_gran(pb + src * (PMSG / 8) + idx);
          gm[k] = ld_gran(pb + src * (PMSG / 8) + 128 + (lane & 1));
        }
        bool ok = true;
#pragma unroll
        for (int k = 0; k < 3; ++k) {
          ok &= ((unsigned)(gv[k] >> 32) == tag) && ((unsigned)(gm[k] >> 32) == tag);
          ov[k] = __uint_as_float((unsigned)gv[k]);
          mv[k] = __builtin_bit_cast(float, __builtin_amdgcn_readlane((int)(unsigned)gm[k], 0));
          lv[k] = __builtin_bit_cast(float, __builtin_amdgcn_readlane((int)(unsigned)gm[k], 1));
        }
        if (__all(ok)) break;
        if (rt() - tstart > TMO) break;
      }
      const float Mx = fmaxf(fmaxf(M, mv[0]), fmaxf(mv[1], mv[2]));
      const float eo = __builtin_amdgcn_exp2f((M - Mx) * L2E), e0 = __builtin_amdgcn_exp2f((mv[0] - Mx) * L2E), e1 = __builtin_amdgcn_exp2f((mv[1] - Mx) * L2E), e2 = __builtin_amdgcn_exp2f((mv[2] - Mx) * L2E);
      const float Lt = eo * L + e0 * lv[0] + e1 * lv[1] + e2 * lv[2];
      const float of = (eo * o + e0 * ov[0] + e1 * ov[1] + e2 * ov[2]) / Lt;
      ((LAS float*)(lds + L_OS))[idx] = rbf(of);
    }
  }
  STP(w < 8 ? 5 : 6);
  BAR();
  if (w < 8) {
    const int rg = lane & 7, cc = (lane >> 3) < 7 ? (lane >> 3) : 6;
    float a[8] = {0, 0, 0, 0, 0, 0, 0, 0};
    const LAS float* os = (const LAS float*)(lds + L_OS) + rg;
    const LAS unsigned char* wp = lds + L_WO + rg * 896 + (7 * w + cc) * 16;
#pragma unroll
    for (int r = 0; r < 16; ++r) {
      const v4u wv = *(const LAS v4u*)(wp + r * 8 * 896);
      const float ov = os[8 * r];
#pragma unroll
      for (int e = 0; e < 4; ++e) { a[2 * e] += ov * bflo(wv[e]); a[2 * e + 1] += ov * bfhi(wv[e]); }
    }
    float y[4];
#pragma unroll
    for (int v = 0; v < 4; ++v) { const float t1 = a[v] + dpp<HMIR>(a[v]), t2 = a[v + 4] + dpp<HMIR>(a[v + 4]); y[v] = (lane & 4) ? t2 : t1; }
    float z[2];
#pragma unroll
    for (int v = 0; v < 2; ++v) { const float t1 = y[v] + dpp<XOR2>(y[v]), t2 = y[v + 2] + dpp<XOR2>(y[v + 2]); z[v] = (lane & 2) ? t2 : t1; }
    const float t1 = z[0] + dpp<XOR1>(z[0]), t2 = z[1] + dpp<XOR1>(z[1]);
    const float s = (lane & 1) ? t2 : t1;
    if (lane < 56) {
      const int col = 56 * w + lane;
      const int dst = col / 14, ci = col - dst * 14;
      st_gran((GAS u64*)(wsg + WS_FS + (((b * 32 + dst) * 32 + j32) * 16 + ci) * 8), tag, __float_as_uint(s));
    }
  }
  if (w == 0) {
    GAS u64* fb = (GAS u64*)(wsg + WS_FS + (b * 32 + j32) * 4096) + lane;
    float s;
    for (;;) {
      asm volatile("" ::: "memory");
      u64 gv[8]; bool ok = true; s = 0.f;
#pragma unroll
      for (int k = 0; k < 8; ++k) gv[k] = ld_gran(fb + k * 64);
#pragma unroll
      for (int k = 0; k < 8; ++k) { ok &= ((unsigned)(gv[k] >> 32) == tag) || ((lane & 15) >= 14); s += __uint_as_float((unsigned)gv[k]); }
      if (__all(ok)) break;
      if (rt() - tstart > TMO) break;
    }
    s = sum16(s); s = sum32(s);
    if (lane < 14) {
      const int col = j32 * 14 + lane;
      const float sg = ((const LAS float*)(lds + L_G))[lane], xv = ((const LAS float*)(lds + L_G))[16 + lane];
      const float r = xv + rbf(rbf(s) * sg);
      out[b * D + col] = (u16)(pk2(r, r) & 0xffffu);
    }
    if (bid == 0 && lane == 0) {
      const unsigned E = __hip_atomic_load((GAS unsigned*)(wsg + WS_EPOCH), RLX_AGENT);
      __hip_atomic_store((GAS unsigned*)(wsg + WS_EPOCH), E + 1u, RLX_AGENT);
    }
  }
  STP(7);
}

extern "C" void kernel_launch(void* const* d_in, const int* in_sizes, int n_in, void* d_out, int out_size, void* d_ws, size_t ws_size, hipStream_t stream) {
  static bool attr_set = false;
  if (!attr_set) { (void)hipFuncSetAttribute((const void*)fused, hipFuncAttributeMaxDynamicSharedMemorySize, L_END); attr_set = true; }
  hipLaunchKernelGGL(fused, dim3(256), dim3(NTHREADS), L_END, stream,
                     (const u16*)d_in[0], (const u16*)d_in[1], (const u16*)d_in[2], (const u16*)d_in[3], (const u16*)d_in[4],
                     (const u16*)d_in[6], (const u16*)d_in[7], (const u16*)d_in[8], (const u16*)d_in[9], (u16*)d_out, (unsigned char*)d_ws);
}
```

```cpp
#define KCOND (b >= 6)
#include <hip/hip_runtime.h>
#include <hip/hip_bf16.h>
#include <stdint.h>

typedef unsigned short u16;
typedef unsigned v4u __attribute__((ext_vector_type(4)));
typedef unsigned v2u __attribute__((ext_vector_type(2)));
typedef float f32x4 __attribute__((ext_vector_type(4)));
typedef short bf16x8 __attribute__((ext_vector_type(8)));
typedef __bf16 bf16x2 __attribute__((ext_vector_type(2)));
typedef unsigned long long u64;
#define LAS __attribute__((address_space(3)))
#define GAS __attribute__((address_space(1)))
#define RLX_AGENT __ATOMIC_RELAXED, __HIP_MEMORY_SCOPE_AGENT

constexpr int D = 448, NQ = 4096, SK = 8192, F = 1792;
#define ABL_W1 0
#define ABL_W2 0
#define ABL_WO 0
#define WDELAY 6
#define POLLSLEEP 16
#define AUXPOL 2
constexpr int NBATCH = 32;
constexpr float L2E = 1.4426950408889634f;
constexpr int NTHREADS = 704;

constexpr unsigned WS_EPOCH = 0;
constexpr unsigned WS_QBOX = 4096;
constexpr unsigned WS_PBOX = WS_QBOX + 8 * 8 * 32 * 64;
constexpr unsigned PMSG = 136 * 8;
constexpr unsigned WS_FS = WS_PBOX + 64 * 16 * PMSG;
constexpr unsigned WS_FH = WS_FS + 8 * 32 * 32 * 128;
constexpr unsigned WS_END = WS_FH + 8 * 32 * 32 * 128;

constexpr int L_WO = 0;
constexpr int SC_STRIDE = 8 * 68;
constexpr int L_SC = L_WO + 128 * 896;
constexpr int L_XN = L_SC + 8 * SC_STRIDE * 4;
constexpr int L_WQ = L_XN + 8 * 896;
constexpr int L_QV = L_WQ + 448 * 32;
constexpr int L_TAG = L_QV + 1024, L_QS = L_TAG + 16, L_OS = L_QS + 256, L_G = L_OS + 512, L_HRED = L_G + 128, L_ML = L_HRED + 8 * 56 * 4, L_CNT = L_ML + 128;
constexpr int L_TS = L_CNT + 16;
constexpr int L_END = L_TS;
constexpr int L_MPART = L_SC;
static_assert(L_END <= 160 * 1024, "lds");

__device__ __forceinline__ float bf2f(u16 b) { return __uint_as_float(((unsigned)b) << 16); }
__device__ __forceinline__ float bflo(unsigned w) { return __uint_as_float(w << 16); }
__device__ __forceinline__ float bfhi(unsigned w) { return __uint_as_float(w & 0xffff0000u); }
__device__ __forceinline__ unsigned pk2(float a, float b) { bf16x2 v = {(__bf16)a, (__bf16)b}; return __builtin_bit_cast(unsigned, v); }
__device__ __forceinline__ float rbf(float f) { return bflo(pk2(f, f)); }
__device__ __forceinline__ float dot2(unsigned k, unsigned q, float acc) {
  return __builtin_amdgcn_fdot2_f32_bf16(__builtin_bit_cast(bf16x2, k), __builtin_bit_cast(bf16x2, q), acc, false);
}
template <int CTRL> __device__ __forceinline__ float dpp(float x) {
  return __builtin_bit_cast(float, __builtin_amdgcn_mov_dpp(__builtin_bit_cast(int, x), CTRL, 0xf, 0xf, true));
}
constexpr int XOR1 = 0xB1, XOR2 = 0x4E, HMIR = 0x141, ROR8 = 0x128;
__device__ __forceinline__ float sum16(float x) {
  auto s = __builtin_amdgcn_permlane16_swap(__float_as_uint(x), __float_as_uint(x), false, false); return __uint_as_float(s[0]) + __uint_as_float(s[1]); }
__device__ __forceinline__ float sum32(float x) {
  auto s = __builtin_amdgcn_permlane32_swap(__float_as_uint(x), __float_as_uint(x), false, false); return __uint_as_float(s[0]) + __uint_as_float(s[1]); }
__device__ __forceinline__ float max16(float x) {
  auto s = __builtin_amdgcn_permlane16_swap(__float_as_uint(x), __float_as_uint(x), false, false); return fmaxf(__uint_as_float(s[0]), __uint_as_float(s[1])); }
__device__ __forceinline__ float max32(float x) {
  auto s = __builtin_amdgcn_permlane32_swap(__float_as_uint(x), __float_as_uint(x), false, false); return fmaxf(__uint_as_float(s[0]), __uint_as_float(s[1])); }
__device__ __forceinline__ float wsum64(float x) {
  x += dpp<XOR1>(x); x += dpp<XOR2>(x); x += dpp<HMIR>(x); x += dpp<ROR8>(x); x = sum16(x); return sum32(x); }
__device__ __forceinline__ f32x4 mfma(v4u a, v4u b, f32x4 c) {
  return __builtin_amdgcn_mfma_f32_16x16x32_bf16(__builtin_bit_cast(bf16x8, a), __builtin_bit_cast(bf16x8, b), c, 0, 0, 0);
}
__device__ __forceinline__ u64 rt() { return __builtin_amdgcn_s_memrealtime(); }
#define WAITV(n) asm volatile("s_waitcnt vmcnt(" #n ")" ::: "memory")
#define WAITL() asm volatile("s_waitcnt lgkmcnt(0)" ::: "memory")
#define SB() __builtin_amdgcn_sched_barrier(0)
#define BAR() do { WAITL(); __builtin_amdgcn_s_barrier(); asm volatile("" ::: "memory"); } while (0)
__device__ __forceinline__ void st_gran(GAS u64* p, unsigned tag, unsigned val) { __hip_atomic_store(p, ((u64)tag << 32) | val, RLX_AGENT); }
__device__ __forceinline__ u64 ld_gran(GAS u64* p) { return __hip_atomic_load(p, RLX_AGENT); }
constexpr u64 TMO = 400000;
constexpr unsigned OOB = 0x7fffff00u;

#define STP(k) do {} while (0)

__global__ __launch_bounds__(NTHREADS) void fused(const u16* __restrict__ x, const u16* __restrict__ kc, const u16* __restrict__ vc, const u16* __restrict__ wq, const u16* __restrict__ wo,
                                             const u16* __restrict__ w1, const u16* __restrict__ b1, const u16* __restrict__ w2, const u16* __restrict__ b2, u16* __restrict__ out, unsigned char* __restrict__ ws) {
  extern __shared__ __attribute__((aligned(16))) unsigned char lds_[];
  LAS unsigned char* lds = (LAS unsigned char*)lds_;
  const int bid = blockIdx.x; const int b = bid >> 5, j32 = bid & 31, kvh = j32 >> 2, sp = bid & 3;
  const int tid = threadIdx.x, lane = tid & 63; const int w = __builtin_amdgcn_readfirstlane(tid >> 6);
  GAS unsigned char* wsg = (GAS unsigned char*)ws;
  STP(0);
  const u64 tstart = rt();
  unsigned tag = 0;
  const size_t kvbase = ((size_t)(b * 8 + kvh) * SK + (size_t)sp * 2048) * 512;
  float o_own = 0.f, M_own = 0.f, L_own = 0.f;

  if (w < 8) {
    const __amdgpu_buffer_rsrc_t rk = __builtin_amdgcn_make_buffer_rsrc((void*)((const char*)kc + kvbase), 0, 1u << 20, 0x00020000);
    const __amdgpu_buffer_rsrc_t rv = __builtin_amdgcn_make_buffer_rsrc((void*)((const char*)vc + kvbase), 0, 1u << 20, 0x00020000);
    const __amdgpu_buffer_rsrc_t rx = __builtin_amdgcn_make_buffer_rsrc((void*)x, 0, 8 * 896, 0x00020000);
    const unsigned koff = lane * 16;
    const unsigned voff = (lane >> 4) * 512 + (lane & 15) * 16;
    auto ldk = [&](int i, v4u (&r)[4]) {
      if (KCOND) {
#pragma unroll
        for (int u = 0; u < 4; ++u) r[u] = __builtin_amdgcn_raw_buffer_load_b128(rk, koff + u * 1024, (unsigned)(i * 8 + w) * 4096u, 0);
      } else {
#pragma unroll
        for (int u = 0; u < 4; ++u) r[u] = __builtin_amdgcn_raw_buffer_load_b128(rk, koff + u * 1024, (unsigned)(i * 8 + w) * 4096u, 2);
      }
    };
    auto ldv = [&](int i, v4u (&r)[4]) {
#pragma unroll
      for (int u = 0; u < 4; ++u) r[u] = __builtin_amdgcn_raw_buffer_load_b128(rv, voff + (u >> 1) * 2048 + (u & 1) * 256, (unsigned)(i * 8 + w) * 4096u, 2);
    };
    v4u K0[4], K1[4], K2[4], K3[4], K4[4], K5[4], K6[4], K7[4];
    const v4u xr = __builtin_amdgcn_raw_buffer_load_b128(rx, lane < 56 ? (unsigned)(w * 896 + lane * 16) : OOB, 0, 0);
    SB();
    ldk(0, K0); SB();
    __builtin_amdgcn_s_sleep(WDELAY);
    const int mrs = lane / 7, mcc = lane - mrs * 7;
    const __amdgpu_buffer_rsrc_t r1 = __builtin_amdgcn_make_buffer_rsrc((void*)w1, 0, D * F * 2, 0x00020000);
    const __amdgpu_buffer_rsrc_t r2 = __builtin_amdgcn_make_buffer_rsrc((void*)(w2 + (size_t)j32 * 56 * D), 0, 56 * 896, 0x00020000);
    v4u W1[7], W2[7];
    {
      const unsigned o1 = lane < 56 ? (unsigned)((56 * w + mrs) * (F * 2) + j32 * 112 + mcc * 16) : OOB;
#pragma unroll
      for (int k = 0; k < 7; ++k) W1[k] = __builtin_amdgcn_raw_buffer_load_b128(r1, ABL_W1 ? OOB : o1, (unsigned)(k * 8 * F * 2), AUXPOL);
      const unsigned o2 = lane < 56 ? (unsigned)(7 * w * 896 + lane * 16) : OOB;
#pragma unroll
      for (int k = 0; k < 7; ++k) W2[k] = __builtin_amdgcn_raw_buffer_load_b128(r2, ABL_W2 ? OOB : o2, (unsigned)(k * 896), AUXPOL);
    }
    const float b1v = bf2f(b1[j32 * 56 + 7 * w + (lane < 7 ? lane : 6)]);
    SB();
    {
      float v[8];
#pragma unroll
      for (int k = 0; k < 4; ++k) { v[2 * k] = bflo(xr[k]); v[2 * k + 1] = bfhi(xr[k]); }
      float s = ((v[0] + v[1]) + (v[2] + v[3])) + ((v[4] + v[5]) + (v[6] + v[7]));
      s = wsum64(s);
      const float mean = s * (1.0f / 448.0f);
      float q = 0.f;
#pragma unroll
      for (int k = 0; k < 8; ++k) { const float d = v[k] - mean; q += d * d; }
      q = lane < 56 ? q : 0.f;
      q = wsum64(q);
      const float meanb = rbf(mean);
      const float den = rbf(sqrtf(rbf(rbf(q * (1.0f / 448.0f)) + 1e-5f)));
      float rc = __builtin_amdgcn_rcpf(den); rc = rc * (2.0f - den * rc);
      v4u o;
#pragma unroll
      for (int k = 0; k < 4; ++k) { const unsigned d2 = pk2(v[2 * k] - meanb, v[2 * k + 1] - meanb); o[k] = pk2(bflo(d2) * rc, bfhi(d2) * rc); }
      if (lane < 56) *(LAS v4u*)(lds + L_XN + w * 896 + lane * 16) = o;
    }
    STP(8);
    BAR();
    tag = *(const LAS unsigned*)(lds + L_TAG);
    volatile LAS unsigned* cnt = (volatile LAS unsigned*)(lds + L_CNT);
    WAITV(15); SB(); STP(9); ldk(1, K1); SB();
    WAITV(12); SB(); STP(11); ldk(2, K2); SB();
    {
      const int rsc = mrs < 8 ? mrs : 7;
      const LAS u16* xn = (const LAS u16*)(lds + L_XN + b * 896) + 56 * w + rsc;
      float acc[8] = {0, 0, 0, 0, 0, 0, 0, 0};
#pragma unroll
      for (int k = 0; k < 7; ++k) {
        const float xv = bf2f(xn[8 * k]);
#pragma unroll
        for (int e = 0; e < 4; ++e) { acc[2 * e] += xv * bflo(W1[k][e]); acc[2 * e + 1] += xv * bfhi(W1[k][e]); }
      }
      LAS float* scr = (LAS float*)(lds + L_SC) + w * SC_STRIDE;
      if (lane < 56) { *(LAS f32x4*)(scr + mrs * 56 + mcc * 8) = f32x4{acc[0], acc[1], acc[2], acc[3]}; *(LAS f32x4*)(scr + mrs * 56 + mcc * 8 + 4) = f32x4{acc[4], acc[5], acc[6], acc[7]}; }
      WAITL();
      const int lc = lane < 56 ? lane : 55;
      float s = 0.f;
#pragma unroll
      for (int r = 0; r < 8; ++r) s += scr[r * 56 + lc];
      if (lane < 56) ((LAS float*)(lds + L_HRED))[w * 56 + lane] = s;
      WAITL();
      if (lane == 0) __hip_atomic_fetch_add((LAS unsigned*)(lds + L_CNT), 1u, __ATOMIC_RELAXED, __HIP_MEMORY_SCOPE_WORKGROUP);
    }
    STP(12);
    WAITV(0); SB(); STP(13); ldk(3, K3); SB();
    WAITV(0); SB(); STP(14); ldk(4, K4); SB();
    {
      while (cnt[0] < 8u) __builtin_amdgcn_s_sleep(1);
      asm volatile("" ::: "memory");
      float hs = 0.f;
      {
        const LAS float* hr = (const LAS float*)(lds + L_HRED) + 7 * w + (lane < 7 ? lane : 6);
#pragma unroll
        for (int ww = 0; ww < 8; ++ww) hs += hr[ww * 56];
      }
      const float t = rbf(rbf(hs) + b1v);
      const float u = 0.7978845608028654f * (t + 0.044715f * t * t * t);
      const float e2 = __builtin_amdgcn_exp2f(u * (2.0f * L2E));
      const float th = 1.0f - 2.0f / (e2 + 1.0f);
      const float hg = rbf(0.5f * t * (1.0f + th));
      float a2[8] = {0, 0, 0, 0, 0, 0, 0, 0};
#pragma unroll
      for (int k = 0; k < 7; ++k) {
        const float hv = __builtin_bit_cast(float, __builtin_amdgcn_readlane(__builtin_bit_cast(int, hg), k));
#pragma unroll
        for (int e = 0; e < 4; ++e) { a2[2 * e] += hv * bflo(W2[k][e]); a2[2 * e + 1] += hv * bfhi(W2[k][e]); }
      }
      LAS float* mp = (LAS float*)(lds + L_MPART) + w * 448 + lane * 8;
      if (lane < 56) { *(LAS f32x4*)(mp) = f32x4{a2[0], a2[1], a2[2], a2[3]}; *(LAS f32x4*)(mp + 4) = f32x4{a2[4], a2[5], a2[6], a2[7]}; }
      WAITL();
      if (lane == 0) __hip_atomic_fetch_add((LAS unsigned*)(lds + L_CNT + 4), 1u, __ATOMIC_RELAXED, __HIP_MEMORY_SCOPE_WORKGROUP);
    }
    WAITV(0); SB(); ldk(5, K5); SB();
    WAITV(0); SB(); STP(16); ldk(6, K6); SB();
    {
      while (cnt[1] < 8u) __builtin_amdgcn_s_sleep(1);
      asm volatile("" ::: "memory");
      if (lane < 56) {
        const int col = 56 * w + lane;
        const LAS float* mp = (const LAS float*)(lds + L_MPART) + col;
        float s = 0.f;
#pragma unroll
        for (int ww = 0; ww < 8; ++ww) s += mp[ww * 448];
        const int dst = col / 14, ci = col - dst * 14;
        st_gran((GAS u64*)(wsg + WS_FH + (((b * 32 + dst) * 32 + j32) * 16 + ci) * 8), tag, __float_as_uint(s));
      }
    }
    STP(15);
    WAITV(0); SB(); ldk(7, K7); SB();
    STP(1);
    BAR();
    STP(2);
    const int c = lane & 31;
    const v4u q0 = *(const LAS v4u*)(lds + L_QV + c * 16), q1 = *(const LAS v4u*)(lds + L_QV + 512 + c * 16);
    LAS float* scw = (LAS float*)(lds + L_SC) + w * SC_STRIDE;
    float mrun = -INFINITY;
    const int hsel = (lane >> 2) & 1, ksel = 2 * ((lane >> 3) & 3) + (lane >> 5);
    auto kcomp = [&](int i, const v4u (&r)[4]) {
      float xx[8];
#pragma unroll
      for (int u = 0; u < 4; ++u) {
        float a = 0.f, bb = 0.f;
#pragma unroll
        for (int k = 0; k < 4; ++k) { a = dot2(r[u][k], q0[k], a); bb = dot2(r[u][k], q1[k], bb); }
        xx[2 * u] = a; xx[2 * u + 1] = bb;
      }
      float y[4];
#pragma unroll
      for (int v = 0; v < 4; ++v) {
        auto s = __builtin_amdgcn_permlane16_swap(__float_as_uint(xx[v]), __float_as_uint(xx[v + 4]), false, false);
        y[v] = __uint_as_float(s[0]) + __uint_as_float(s[1]);
      }
      float z[2];
#pragma unroll
      for (int v = 0; v < 2; ++v) {
        const float t1 = y[v] + dpp<ROR8>(y[v]), t2 = y[v + 2] + dpp<ROR8>(y[v + 2]);
        z[v] = (lane & 8) ? t2 : t1;
      }
      const float t1 = z[0] + dpp<HMIR>(z[0]), t2 = z[1] + dpp<HMIR>(z[1]);
      float s = (lane & 4) ? t2 : t1;
      s += dpp<XOR2>(s); s += dpp<XOR1>(s);
      s = rbf(s) * 0.0625f;
      mrun = fmaxf(mrun, s);
      if ((lane & 3) == 0) scw[hsel * 256 + i * 8 + ksel] = s;
    };
    kcomp(0, K0); SB(); kcomp(1, K1); SB(); kcomp(2, K2); SB(); kcomp(3, K3); SB(); kcomp(4, K4); SB(); kcomp(5, K5); SB(); kcomp(6, K6); SB();
#pragma unroll 1
    for (int i = 7; i < NBATCH - 1; i += 2) {
      WAITV(0); SB();
      ldk(i + 1, K0); SB();
      kcomp(i, K7); SB();
      WAITV(0); SB();
      ldk(i + 2, K7); SB();
      kcomp(i + 1, K0); SB();
    }
    WAITV(0); SB();
    ldv(0, K0); SB();
    kcomp(NBATCH - 1, K7); SB();
    STP(3);
    mrun = fmaxf(mrun, dpp<ROR8>(mrun)); mrun = max16(mrun); mrun = max32(mrun);
    const float m0 = __builtin_bit_cast(float, __builtin_amdgcn_readlane(__builtin_bit_cast(int, mrun), 0));
    const float m1 = __builtin_bit_cast(float, __builtin_amdgcn_readlane(__builtin_bit_cast(int, mrun), 4));
    const int hh = lane >> 5;
    const float mh = hh ? m1 : m0;
    float lsum;
    {
      WAITL();
      const f32x4 s0 = *(LAS f32x4*)(scw + hh * 256 + c * 8), s1 = *(LAS f32x4*)(scw + hh * 256 + c * 8 + 4);
      WAITL();
      unsigned pw[8]; lsum = 0.f;
#pragma unroll
      for (int e = 0; e < 4; ++e) {
        const float p0 = __builtin_amdgcn_exp2f((s0[e] - mh) * L2E), p1 = __builtin_amdgcn_exp2f((s1[e] - mh) * L2E);
        lsum += p0 + p1;
        pw[e] = pk2(p0, p0); pw[e + 4] = pk2(p1, p1);
      }
      LAS unsigned* pwl = (LAS unsigned*)scw;
#pragma unroll
      for (int g = 0; g < 4; ++g) *(LAS v2u*)(pwl + (hh * 4 + g) * 68 + 2 * c) = v2u{pw[g], pw[g + 4]};
      lsum += dpp<XOR1>(lsum); lsum += dpp<XOR2>(lsum); lsum += dpp<HMIR>(lsum); lsum += dpp<ROR8>(lsum); lsum = sum16(lsum);
      WAITL();
    }
    f32x4 acc0 = {0, 0, 0, 0}, acc1 = {0, 0, 0, 0};
    const int ii = lane & 15, g = lane >> 4, hV = ii >> 3, jj = ii & 7;
    unsigned msk[4];
#pragma unroll
    for (int r = 0; r < 4; ++r) msk[r] = (r == (jj >> 1)) ? ((jj & 1) ? 0xffff0000u : 0x0000ffffu) : 0u;
    const LAS unsigned* prd = (const LAS unsigned*)scw + (hV * 4 + g) * 68;
    auto vcomp = [&](int i, const v4u (&r)[4]) {
      const v2u pw = *(const LAS v2u*)(prd + 2 * i);
#pragma unroll
      for (int rg = 0; rg < 2; ++rg) {
        const unsigned pv = pw[rg];
        const v4u a = {pv & msk[0], pv & msk[1], pv & msk[2], pv & msk[3]};
        acc0 = mfma(a, r[2 * rg], acc0);
        acc1 = mfma(a, r[2 * rg + 1], acc1);
      }
    };
#pragma unroll 1
    for (int i = 0; i < NBATCH; i += 2) {
      WAITV(0); SB();
      ldv(i + 1, K7); SB();
      vcomp(i, K0); SB();
      WAITV(0); SB();
      if (i + 2 < NBATCH) ldv(i + 2, K0);
      SB();
      vcomp(i + 1, K7); SB();
    }
    STP(4);
    LAS float* accw = (LAS float*)(lds + L_SC) + w * SC_STRIDE + hh * 256 + 8 * (lane & 15) + 4 * ((lane >> 4) & 1);
    *(LAS f32x4*)(accw) = acc0; *(LAS f32x4*)(accw + 128) = acc1;
    LAS float* ml = (LAS float*)(lds + L_ML) + w * 4;
    if (c == 0) { ml[hh * 2] = mh; ml[hh * 2 + 1] = lsum; }
  } else if (w == 8) {
    const unsigned E = __hip_atomic_load((GAS unsigned*)(wsg + WS_EPOCH), RLX_AGENT);
    const int pidx = sp * 8 + b;
    {
      const u16* src = wq + (size_t)(lane >> 1) * NQ + kvh * 512 + pidx * 16 + (lane & 1) * 8;
#pragma unroll
      for (int it = 0; it < 14; ++it)
        __builtin_amdgcn_global_load_lds((const unsigned*)(src + (size_t)it * 32 * NQ), (unsigned*)(lds_ + L_WQ + it * 1024), 16, 0, AUXPOL);
    }
    unsigned warm;
    {
      const GAS unsigned char* p;
      if (lane < 16) p = (const GAS unsigned char*)vc + kvbase + (size_t)lane * 65536;
      else if (lane < 32) p = (const GAS unsigned char*)kc + kvbase + (size_t)(lane - 16) * 65536;
      else if (lane < 40) p = wsg + WS_PBOX + (size_t)(b * 8 + kvh) * 16 * PMSG + (lane - 32) * 2176;
      else if (lane < 48) p = wsg + WS_FS + (size_t)b * 131072 + (lane - 40) * 16384;
      else if (lane < 56) p = wsg + WS_FH + (size_t)b * 131072 + (lane - 48) * 16384;
      else p = (const GAS unsigned char*)wo + ((size_t)(kvh * 512 + sp * 128)) * 896 + (lane - 56) * 14336;
      warm = __builtin_nontemporal_load((const GAS unsigned*)p);
    }
    tag = E * 0x9E3779B1u + 0x7F4A7C15u;
    if (lane < 4) ((LAS unsigned*)(lds + L_CNT))[lane] = 0u;
    if (lane == 0) *(LAS unsigned*)(lds + L_TAG) = tag;
    BAR();
    WAITV(1);
    STP(10);
    {
      const int i16 = lane & 15, g = lane >> 4;
      const unsigned aaddr = L_XN + (i16 & 7) * 896 + g * 16;
      const unsigned baddr = (unsigned)(size_t)(lds) + L_WQ + (8 * g + (i16 >> 2)) * 32 + (i16 & 3) * 8;
      v4u a[14]; v2u b0[14], b1v[14];
#pragma unroll
      for (int ks = 0; ks < 14; ++ks) a[ks] = *(const LAS v4u*)(lds + aaddr + ks * 64);
#pragma unroll
      for (int ks = 0; ks < 14; ++ks)
        asm volatile("ds_read_b64_tr_b16 %0, %2 offset:%3\n\tds_read_b64_tr_b16 %1, %2 offset:%4" : "=&v"(b0[ks]), "=&v"(b1v[ks]) : "v"(baddr), "i"(ks * 1024), "i"(ks * 1024 + 128) : "memory");
      asm volatile("s_waitcnt lgkmcnt(0)" : "+v"(b0[0]), "+v"(b0[1]), "+v"(b0[2]), "+v"(b0[3]), "+v"(b0[4]), "+v"(b0[5]), "+v"(b0[6]), "+v"(b0[7]), "+v"(b0[8]), "+v"(b0[9]), "+v"(b0[10]), "+v"(b0[11]), "+v"(b0[12]), "+v"(b0[13]) :: "memory");
      asm volatile("" : "+v"(b1v[0]), "+v"(b1v[1]), "+v"(b1v[2]), "+v"(b1v[3]), "+v"(b1v[4]), "+v"(b1v[5]), "+v"(b1v[6]), "+v"(b1v[7]), "+v"(b1v[8]), "+v"(b1v[9]), "+v"(b1v[10]), "+v"(b1v[11]), "+v"(b1v[12]), "+v"(b1v[13]) :: "memory");
      SB();
      f32x4 dq0 = {0, 0, 0, 0}, dq1 = {0, 0, 0, 0};
#pragma unroll
      for (int ks = 0; ks < 14; ks += 2) {
        dq0 = mfma(a[ks], v4u{b0[ks][0], b0[ks][1], b1v[ks][0], b1v[ks][1]}, dq0);
        dq1 = mfma(a[ks + 1], v4u{b0[ks + 1][0], b0[ks + 1][1], b1v[ks + 1][0], b1v[ks + 1][1]}, dq1);
      }
      const f32x4 dq = dq0 + dq1;
      LAS u16* qs = (LAS u16*)(lds + L_QS);
      if (g < 2) {
#pragma unroll
        for (int r = 0; r < 4; ++r) qs[(4 * g + r) * 16 + i16] = (u16)(pk2(dq[r], dq[r]) & 0xffffu);
      }
      WAITL();
      const unsigned qw = *(const LAS unsigned*)(lds + L_QS + lane * 4);
      st_gran((GAS u64*)(wsg + WS_QBOX + (((kvh * 8 + (lane >> 3)) * 32 + pidx) * 8 + (lane & 7)) * 8), tag, qw);
      if (lane == 0) ((volatile LAS unsigned*)(lds + L_CNT))[2] = 1u;
    }
    STP(1);
    {
      const __amdgpu_buffer_rsrc_t rq = __builtin_amdgcn_make_buffer_rsrc((void*)(ws + WS_QBOX + (kvh * 8 + b) * 2048), 0, 2048, 0x00020000);
      v4u g0, g1; int npoll = 0;
      for (;;) {
        asm volatile("" ::: "memory");
        ++npoll;
        __builtin_amdgcn_s_sleep(POLLSLEEP);
        g0 = __builtin_amdgcn_raw_buffer_load_b128(rq, lane * 32, 0, 16);
        g1 = __builtin_amdgcn_raw_buffer_load_b128(rq, lane * 32 + 16, 0, 16);
        const bool ok = g0[1] == tag && g0[3] == tag && g1[1] == tag && g1[3] == tag;
        if (__all(ok)) break;
        if (rt() - tstart > TMO) break;
      }
      *(LAS v4u*)(lds + L_QV + lane * 16) = v4u{g0[0], g0[2], g1[0], g1[2]};
    }
    STP(2);
    BAR();
    asm volatile("" :: "v"(warm));
    {
      GAS u64* fb = (GAS u64*)(wsg + WS_FH + (b * 32 + j32) * 4096) + lane;
      float s = 0.f;
      for (;;) {
        __builtin_amdgcn_s_sleep(64);
        asm volatile("" ::: "memory");
        bool ok = true; s = 0.f;
        u64 gv[8];
#pragma unroll
        for (int k = 0; k < 8; ++k) gv[k] = ld_gran(fb + k * 64);
#pragma unroll
        for (int k = 0; k < 8; ++k) { ok &= ((unsigned)(gv[k] >> 32) == tag) || ((lane & 15) >= 14); s += __uint_as_float((unsigned)gv[k]); }
        if (__all(ok)) break;
        if (rt() - tstart > TMO) break;
      }
      s = sum16(s); s = sum32(s);
      if (lane < 14) {
        const int col = j32 * 14 + lane;
        const float gg = rbf(rbf(s) + bf2f(b2[col]));
        const float sg = rbf(gg / (1.0f + __builtin_amdgcn_exp2f(-gg * L2E)));
        LAS float* gl = (LAS float*)(lds + L_G);
        gl[lane] = sg;
        gl[16 + lane] = bf2f(x[b * D + col]);
      }
    }
    STP(5);
  } else {
    {
      const int half = w - 9;
      const u16* src = wo + ((size_t)(kvh * 512 + sp * 128)) * D + half * 56 * 512 + lane * 8;
      __builtin_amdgcn_s_sleep(WDELAY);
#pragma unroll
      for (int k = 0; k < (ABL_WO ? 0 : 56); ++k)
        __builtin_amdgcn_global_load_lds((const unsigned*)(src + (size_t)k * 512), (unsigned*)(lds_ + L_WO + (half * 56 + k) * 1024), 16, 0, AUXPOL);
      BAR();
      BAR();
      WAITV(0);
    }
  }
  BAR();
  const int pair = b * 8 + kvh;
  if (w < 8) {
    const int h = tid >> 8;
    const LAS float* ml = (const LAS float*)(lds + L_ML);
    float M = -INFINITY;
#pragma unroll
    for (int ww = 0; ww < 8; ++ww) M = fmaxf(M, ml[ww * 4 + h * 2]);
    float L = 0.f, o = 0.f;
    const LAS float* ac = (const LAS float*)(lds + L_SC) + tid;
#pragma unroll
    for (int ww = 0; ww < 8; ++ww) { const float sc = __builtin_amdgcn_exp2f((ml[ww * 4 + h * 2] - M) * L2E); L += sc * ml[ww * 4 + h * 2 + 1]; o += sc * ac[ww * SC_STRIDE]; }
    const int dst = tid >> 7, idx = tid & 127;
    if (dst != sp) {
      GAS u64* pb = (GAS u64*)(wsg + WS_PBOX + ((pair * 4 + dst) * 4 + sp) * PMSG);
      st_gran(pb + idx, tag, __float_as_uint(o));
      if (idx == 0) { st_gran(pb + 128, tag, __float_as_uint(M)); st_gran(pb + 129, tag, __float_as_uint(L)); }
    } else {
      GAS u64* pb = (GAS u64*)(wsg + WS_PBOX + ((pair * 4 + sp) * 4) * PMSG);
      float ov[3], mv[3], lv[3];
      for (;;) {
        asm volatile("" ::: "memory");
        u64 gv[3], gm[3];
#pragma unroll
        for (int k = 0; k < 3; ++k) {
          const int src = (sp + 1 + k) & 3;
          gv[k] = ld_gran(pb + src * (PMSG / 8) + idx);
          gm[k] = ld_gran(pb + src * (PMSG / 8) + 128 + (lane & 1));
        }
        bool ok = true;
#pragma unroll
        for (int k = 0; k < 3; ++k) {
          ok &= ((unsigned)(gv[k] >> 32) == tag) && ((unsigned)(gm[k] >> 32) == tag);
          ov[k] = __uint_as_float((unsigned)gv[k]);
          mv[k] = __builtin_bit_cast(float, __builtin_amdgcn_readlane((int)(unsigned)gm[k], 0));
          lv[k] = __builtin_bit_cast(float, __builtin_amdgcn_readlane((int)(unsigned)gm[k], 1));
        }
        if (__all(ok)) break;
        if (rt() - tstart > TMO) break;
      }
      const float Mx = fmaxf(fmaxf(M, mv[0]), fmaxf(mv[1], mv[2]));
      const float eo = __builtin_amdgcn_exp2f((M - Mx) * L2E), e0 = __builtin_amdgcn_exp2f((mv[0] - Mx) * L2E), e1 = __builtin_amdgcn_exp2f((mv[1] - Mx) * L2E), e2 = __builtin_amdgcn_exp2f((mv[2] - Mx) * L2E);
      const float Lt = eo * L + e0 * lv[0] + e1 * lv[1] + e2 * lv[2];
      const float of = (eo * o + e0 * ov[0] + e1 * ov[1] + e2 * ov[2]) / Lt;
      ((LAS float*)(lds + L_OS))[idx] = rbf(of);
    }
  }
  STP(w < 8 ? 5 : 6);
  BAR();
  if (w < 8) {
    const int rg = lane & 7, cc = (lane >> 3) < 7 ? (lane >> 3) : 6;
    float a[8] = {0, 0, 0, 0, 0, 0, 0, 0};
    const LAS float* os = (const LAS float*)(lds + L_OS) + rg;
    const LAS unsigned char* wp = lds + L_WO + rg * 896 + (7 * w + cc) * 16;
#pragma unroll
    for (int r = 0; r < 16; ++r) {
      const v4u wv = *(const LAS v4u*)(wp + r * 8 * 896);
      const float ov = os[8 * r];
#pragma unroll
      for (int e = 0; e < 4; ++e) { a[2 * e] += ov * bflo(wv[e]); a[2 * e + 1] += ov * bfhi(wv[e]); }
    }
    float y[4];
#pragma unroll
    for (int v = 0; v < 4; ++v) { const float t1 = a[v] + dpp<HMIR>(a[v]), t2 = a[v + 4] + dpp<HMIR>(a[v + 4]); y[v] = (lane & 4) ? t2 : t1; }
    float z[2];
#pragma unroll
    for (int v = 0; v < 2; ++v) { const float t1 = y[v] + dpp<XOR2>(y[v]), t2 = y[v + 2] + dpp<XOR2>(y[v + 2]); z[v] = (lane & 2) ? t2 : t1; }
    const float t1 = z[0] + dpp<XOR1>(z[0]), t2 = z[1] + dpp<XOR1>(z[1]);
    const float s = (lane & 1) ? t2 : t1;
    if (lane < 56) {
      const int col = 56 * w + lane;
      const int dst = col / 14, ci = col - dst * 14;
      st_gran((GAS u64*)(wsg + WS_FS + (((b * 32 + dst) * 32 + j32) * 16 + ci) * 8), tag, __float_as_uint(s));
    }
  }
  if (w == 0) {
    GAS u64* fb = (GAS u64*)(wsg + WS_FS + (b * 32 + j32) * 4096) + lane;
    float s;
    for (;;) {
      asm volatile("" ::: "memory");
      u64 gv[8]; bool ok = true; s = 0.f;
#pragma unroll
      for (int k = 0; k < 8; ++k) gv[k] = ld_gran(fb + k * 64);
#pragma unroll
      for (int k = 0; k < 8; ++k) { ok &= ((unsigned)(gv[k] >> 32) == tag) || ((lane & 15) >= 14); s += __uint_as_float((unsigned)gv[k]); }
      if (__all(ok)) break;
      if (rt() - tstart > TMO) break;
    }
    s = sum16(s); s = sum32(s);
    if (lane < 14) {
      const int col = j32 * 14 + lane;
      const float sg = ((const LAS float*)(lds + L_G))[lane], xv = ((const LAS float*)(lds + L_G))[16 + lane];
      const float r = xv + rbf(rbf(s) * sg);
      out[b * D + col] = (u16)(pk2(r, r) & 0xffffu);
    }
    if (bid == 0 && lane == 0) {
      const unsigned E = __hip_atomic_load((GAS unsigned*)(wsg + WS_EPOCH), RLX_AGENT);
      __hip_atomic_store((GAS unsigned*)(wsg + WS_EPOCH), E + 1u, RLX_AGENT);
    }
  }
  STP(7);
}

extern "C" void kernel_launch(void* const* d_in, const int* in_sizes, int n_in, void* d_out, int out_size, void* d_ws, size_t ws_size, hipStream_t stream) {
  static bool attr_set = false;
  if (!attr_set) { (void)hipFuncSetAttribute((const void*)fused, hipFuncAttributeMaxDynamicSharedMemorySize, L_END); attr_set = true; }
  hipLaunchKernelGGL(fused, dim3(256), dim3(NTHREADS), L_END, stream,
                     (const u16*)d_in[0], (const u16*)d_in[1], (const u16*)d_in[2], (const u16*)d_in[3], (const u16*)d_in[4],
                     (const u16*)d_in[6], (const u16*)d_in[7], (const u16*)d_in[8], (const u16*)d_in[9], (u16*)d_out, (unsigned char*)d_ws);
}
```

```cpp
#define CODEPF 5500
#include <hip/hip_runtime.h>
#include <hip/hip_bf16.h>
#include <stdint.h>

typedef unsigned short u16;
typedef unsigned v4u __attribute__((ext_vector_type(4)));
typedef unsigned v2u __attribute__((ext_vector_type(2)));
typedef float f32x4 __attribute__((ext_vector_type(4)));
typedef short bf16x8 __attribute__((ext_vector_type(8)));
typedef __bf16 bf16x2 __attribute__((ext_vector_type(2)));
typedef unsigned long long u64;
#define LAS __attribute__((address_space(3)))
#define GAS __attribute__((address_space(1)))
#define RLX_AGENT __ATOMIC_RELAXED, __HIP_MEMORY_SCOPE_AGENT

constexpr int D = 448, NQ = 4096, SK = 8192, F = 1792;
#define ABL_W1 0
#define ABL_W2 0
#define ABL_WO 0
#define WDELAY 6
#define POLLSLEEP 16
#define AUXPOL 2
constexpr int NBATCH = 32;
constexpr float L2E = 1.4426950408889634f;
constexpr int NTHREADS = 704;

constexpr unsigned WS_EPOCH = 0;
constexpr unsigned WS_QBOX = 4096;
constexpr unsigned WS_PBOX = WS_QBOX + 8 * 8 * 32 * 64;
constexpr unsigned PMSG = 136 * 8;
constexpr unsigned WS_FS = WS_PBOX + 64 * 16 * PMSG;
constexpr unsigned WS_FH = WS_FS + 8 * 32 * 32 * 128;
constexpr unsigned WS_END = WS_FH + 8 * 32 * 32 * 128;

constexpr int L_WO = 0;
constexpr int SC_STRIDE = 8 * 68;
constexpr int L_SC = L_WO + 128 * 896;
constexpr int L_XN = L_SC + 8 * SC_STRIDE * 4;
constexpr int L_WQ = L_XN + 8 * 896;
constexpr int L_QV = L_WQ + 448 * 32;
constexpr int L_TAG = L_QV + 1024, L_QS = L_TAG + 16, L_OS = L_QS + 256, L_G = L_OS + 512, L_HRED = L_G + 128, L_ML = L_HRED + 8 * 56 * 4, L_CNT = L_ML + 128;
constexpr int L_TS = L_CNT + 16;
constexpr int L_END = L_TS;
constexpr int L_MPART = L_SC;
static_assert(L_END <= 160 * 1024, "lds");

__device__ __forceinline__ float bf2f(u16 b) { return __uint_as_float(((unsigned)b) << 16); }
__device__ __forceinline__ float bflo(unsigned w) { return __uint_as_float(w << 16); }
__device__ __forceinline__ float bfhi(unsigned w) { return __uint_as_float(w & 0xffff0000u); }
__device__ __forceinline__ unsigned pk2(float a, float b) { bf16x2 v = {(__bf16)a, (__bf16)b}; return __builtin_bit_cast(unsigned, v); }
__device__ __forceinline__ float rbf(float f) { return bflo(pk2(f, f)); }
__device__ __forceinline__ float dot2(unsigned k, unsigned q, float acc) {
  return __builtin_amdgcn_fdot2_f32_bf16(__builtin_bit_cast(bf16x2, k), __builtin_bit_cast(bf16x2, q), acc, false);
}
template <int CTRL> __device__ __forceinline__ float dpp(float x) {
  return __builtin_bit_cast(float, __builtin_amdgcn_mov_dpp(__builtin_bit_cast(int, x), CTRL, 0xf, 0xf, true));
}
constexpr int XOR1 = 0xB1, XOR2 = 0x4E, HMIR = 0x141, ROR8 = 0x128;
__device__ __forceinline__ float sum16(float x) {
  auto s = __builtin_amdgcn_permlane16_swap(__float_as_uint(x), __float_as_uint(x), false, false); return __uint_as_float(s[0]) + __uint_as_float(s[1]); }
__device__ __forceinline__ float sum32(float x) {
  auto s = __builtin_amdgcn_permlane32_swap(__float_as_uint(x), __float_as_uint(x), false, false); return __uint_as_float(s[0]) + __uint_as_float(s[1]); }
__device__ __forceinline__ float max16(float x) {
  auto s = __builtin_amdgcn_permlane16_swap(__float_as_uint(x), __float_as_uint(x), false, false); return fmaxf(__uint_as_float(s[0]), __uint_as_float(s[1])); }
__device__ __forceinline__ float max32(float x) {
  auto s = __builtin_amdgcn_permlane32_swap(__float_as_uint(x), __float_as_uint(x), false, false); return fmaxf(__uint_as_float(s[0]), __uint_as_float(s[1])); }
__device__ __forceinline__ float wsum64(float x) {
  x += dpp<XOR1>(x); x += dpp<XOR2>(x); x += dpp<HMIR>(x); x += dpp<ROR8>(x); x = sum16(x); return sum32(x); }
__device__ __forceinline__ f32x4 mfma(v4u a, v4u b, f32x4 c) {
  return __builtin_amdgcn_mfma_f32_16x16x32_bf16(__builtin_bit_cast(bf16x8, a), __builtin_bit_cast(bf16x8, b), c, 0, 0, 0);
}
__device__ __forceinline__ u64 rt() { return __builtin_amdgcn_s_memrealtime(); }
#define WAITV(n) asm volatile("s_waitcnt vmcnt(" #n ")" ::: "memory")
#define WAITL() asm volatile("s_waitcnt lgkmcnt(0)" ::: "memory")
#define SB() __builtin_amdgcn_sched_barrier(0)
#define BAR() do { WAITL(); __builtin_amdgcn_s_barrier(); asm volatile("" ::: "memory"); } while (0)
__device__ __forceinline__ void st_gran(GAS u64* p, unsigned tag, unsigned val) { __hip_atomic_store(p, ((u64)tag << 32) | val, RLX_AGENT); }
__device__ __forceinline__ u64 ld_gran(GAS u64* p) { return __hip_atomic_load(p, RLX_AGENT); }
constexpr u64 TMO = 400000;
constexpr unsigned OOB = 0x7fffff00u;

#define STP(k) do {} while (0)

__global__ __launch_bounds__(NTHREADS) void fused(const u16* __restrict__ x, const u16* __restrict__ kc, const u16* __restrict__ vc, const u16* __restrict__ wq, const u16* __restrict__ wo,
                                             const u16* __restrict__ w1, const u16* __restrict__ b1, const u16* __restrict__ w2, const u16* __restrict__ b2, u16* __restrict__ out, unsigned char* __restrict__ ws) {
  extern __shared__ __attribute__((aligned(16))) unsigned char lds_[];
  LAS unsigned char* lds = (LAS unsigned char*)lds_;
  const int bid = blockIdx.x; const int b = bid >> 5, j32 = bid & 31, kvh = j32 >> 2, sp = bid & 3;
  const int tid = threadIdx.x, lane = tid & 63; const int w = __builtin_amdgcn_readfirstlane(tid >> 6);
  GAS unsigned char* wsg = (GAS unsigned char*)ws;
  asm volatile("fused_code_begin:");
  STP(0);
  const u64 tstart = rt();
  unsigned tag = 0;
  const size_t kvbase = ((size_t)(b * 8 + kvh) * SK + (size_t)sp * 2048) * 512;
  float o_own = 0.f, M_own = 0.f, L_own = 0.f;

  if (w < 8) {
    const __amdgpu_buffer_rsrc_t rk = __builtin_amdgcn_make_buffer_rsrc((void*)((const char*)kc + kvbase), 0, 1u << 20, 0x00020000);
    const __amdgpu_buffer_rsrc_t rv = __builtin_amdgcn_make_buffer_rsrc((void*)((const char*)vc + kvbase), 0, 1u << 20, 0x00020000);
    const __amdgpu_buffer_rsrc_t rx = __builtin_amdgcn_make_buffer_rsrc((void*)x, 0, 8 * 896, 0x00020000);
    const unsigned koff = lane * 16;
    const unsigned voff = (lane >> 4) * 512 + (lane & 15) * 16;
    auto ldk = [&](int i, v4u (&r)[4]) {
#pragma unroll
      for (int u = 0; u < 4; ++u) r[u] = __builtin_amdgcn_raw_buffer_load_b128(rk, koff + u * 1024, (unsigned)(i * 8 + w) * 4096u, 2);
    };
    auto ldv = [&](int i, v4u (&r)[4]) {
#pragma unroll
      for (int u = 0; u < 4; ++u) r[u] = __builtin_amdgcn_raw_buffer_load_b128(rv, voff + (u >> 1) * 2048 + (u & 1) * 256, (unsigned)(i * 8 + w) * 4096u, 2);
    };
    v4u K0[4], K1[4], K2[4], K3[4], K4[4], K5[4], K6[4], K7[4];
    const v4u xr = __builtin_amdgcn_raw_buffer_load_b128(rx, lane < 56 ? (unsigned)(w * 896 + lane * 16) : OOB, 0, 0);
    SB();
    ldk(0, K0); SB();
    __builtin_amdgcn_s_sleep(WDELAY);
    const int mrs = lane / 7, mcc = lane - mrs * 7;
    const __amdgpu_buffer_rsrc_t r1 = __builtin_amdgcn_make_buffer_rsrc((void*)w1, 0, D * F * 2, 0x00020000);
    const __amdgpu_buffer_rsrc_t r2 = __builtin_amdgcn_make_buffer_rsrc((void*)(w2 + (size_t)j32 * 56 * D), 0, 56 * 896, 0x00020000);
    v4u W1[7], W2[7];
    {
      const unsigned o1 = lane < 56 ? (unsigned)((56 * w + mrs) * (F * 2) + j32 * 112 + mcc * 16) : OOB;
#pragma unroll
      for (int k = 0; k < 7; ++k) W1[k] = __builtin_amdgcn_raw_buffer_load_b128(r1, ABL_W1 ? OOB : o1, (unsigned)(k * 8 * F * 2), AUXPOL);
      const unsigned o2 = lane < 56 ? (unsigned)(7 * w * 896 + lane * 16) : OOB;
#pragma unroll
      for (int k = 0; k < 7; ++k) W2[k] = __builtin_amdgcn_raw_buffer_load_b128(r2, ABL_W2 ? OOB : o2, (unsigned)(k * 896), AUXPOL);
    }
    const float b1v = bf2f(b1[j32 * 56 + 7 * w + (lane < 7 ? lane : 6)]);
    SB();
    {
      float v[8];
#pragma unroll
      for (int k = 0; k < 4; ++k) { v[2 * k] = bflo(xr[k]); v[2 * k + 1] = bfhi(xr[k]); }
      float s = ((v[0] + v[1]) + (v[2] + v[3])) + ((v[4] + v[5]) + (v[6] + v[7]));
      s = wsum64(s);
      const float mean = s * (1.0f / 448.0f);
      float q = 0.f;
#pragma unroll
      for (int k = 0; k < 8; ++k) { const float d = v[k] - mean; q += d * d; }
      q = lane < 56 ? q : 0.f;
      q = wsum64(q);
      const float meanb = rbf(mean);
      const float den = rbf(sqrtf(rbf(rbf(q * (1.0f / 448.0f)) + 1e-5f)));
      float rc = __builtin_amdgcn_rcpf(den); rc = rc * (2.0f - den * rc);
      v4u o;
#pragma unroll
      for (int k = 0; k < 4; ++k) { const unsigned d2 = pk2(v[2 * k] - meanb, v[2 * k + 1] - meanb); o[k] = pk2(bflo(d2) * rc, bfhi(d2) * rc); }
      if (lane < 56) *(LAS v4u*)(lds + L_XN + w * 896 + lane * 16) = o;
    }
    STP(8);
    BAR();
    tag = *(const LAS unsigned*)(lds + L_TAG);
    volatile LAS unsigned* cnt = (volatile LAS unsigned*)(lds + L_CNT);
    WAITV(15); SB(); STP(9); ldk(1, K1); SB();
    WAITV(12); SB(); STP(11); ldk(2, K2); SB();
    {
      const int rsc = mrs < 8 ? mrs : 7;
      const LAS u16* xn = (const LAS u16*)(lds + L_XN + b * 896) + 56 * w + rsc;
      float acc[8] = {0, 0, 0, 0, 0, 0, 0, 0};
#pragma unroll
      for (int k = 0; k < 7; ++k) {
        const float xv = bf2f(xn[8 * k]);
#pragma unroll
        for (int e = 0; e < 4; ++e) { acc[2 * e] += xv * bflo(W1[k][e]); acc[2 * e + 1] += xv * bfhi(W1[k][e]); }
      }
      LAS float* scr = (LAS float*)(lds + L_SC) + w * SC_STRIDE;
      if (lane < 56) { *(LAS f32x4*)(scr + mrs * 56 + mcc * 8) = f32x4{acc[0], acc[1], acc[2], acc[3]}; *(LAS f32x4*)(scr + mrs * 56 + mcc * 8 + 4) = f32x4{acc[4], acc[5], acc[6], acc[7]}; }
      WAITL();
      const int lc = lane < 56 ? lane : 55;
      float s = 0.f;
#pragma unroll
      for (int r = 0; r < 8; ++r) s += scr[r * 56 + lc];
      if (lane < 56) ((LAS float*)(lds + L_HRED))[w * 56 + lane] = s;
      WAITL();
      if (lane == 0) __hip_atomic_fetch_add((LAS unsigned*)(lds + L_CNT), 1u, __ATOMIC_RELAXED, __HIP_MEMORY_SCOPE_WORKGROUP);
    }
    STP(12);
    WAITV(0); SB(); STP(13); ldk(3, K3); SB();
    WAITV(0); SB(); STP(14); ldk(4, K4); SB();
    {
      while (cnt[0] < 8u) __builtin_amdgcn_s_sleep(1);
      asm volatile("" ::: "memory");
      float hs = 0.f;
      {
        const LAS float* hr = (const LAS float*)(lds + L_HRED) + 7 * w + (lane < 7 ? lane : 6);
#pragma unroll
        for (int ww = 0; ww < 8; ++ww) hs += hr[ww * 56];
      }
      const float t = rbf(rbf(hs) + b1v);
      const float u = 0.7978845608028654f * (t + 0.044715f * t * t * t);
      const float e2 = __builtin_amdgcn_exp2f(u * (2.0f * L2E));
      const float th = 1.0f - 2.0f / (e2 + 1.0f);
      const float hg = rbf(0.5f * t * (1.0f + th));
      float a2[8] = {0, 0, 0, 0, 0, 0, 0, 0};
#pragma unroll
      for (int k = 0; k < 7; ++k) {
        const float hv = __builtin_bit_cast(float, __builtin_amdgcn_readlane(__builtin_bit_cast(int, hg), k));
#pragma unroll
        for (int e = 0; e < 4; ++e) { a2[2 * e] += hv * bflo(W2[k][e]); a2[2 * e + 1] += hv * bfhi(W2[k][e]); }
      }
      LAS float* mp = (LAS float*)(lds + L_MPART) + w * 448 + lane * 8;
      if (lane < 56) { *(LAS f32x4*)(mp) = f32x4{a2[0], a2[1], a2[2], a2[3]}; *(LAS f32x4*)(mp + 4) = f32x4{a2[4], a2[5], a2[6], a2[7]}; }
      WAITL();
      if (lane == 0) __hip_atomic_fetch_add((LAS unsigned*)(lds + L_CNT + 4), 1u, __ATOMIC_RELAXED, __HIP_MEMORY_SCOPE_WORKGROUP);
    }
    WAITV(0); SB(); ldk(5, K5); SB();
    WAITV(0); SB(); STP(16); ldk(6, K6); SB();
    {
      while (cnt[1] < 8u) __builtin_amdgcn_s_sleep(1);
      asm volatile("" ::: "memory");
      if (lane < 56) {
        const int col = 56 * w + lane;
        const LAS float* mp = (const LAS float*)(lds + L_MPART) + col;
        float s = 0.f;
#pragma unroll
        for (int ww = 0; ww < 8; ++ww) s += mp[ww * 448];
        const int dst = col / 14, ci = col - dst * 14;
        st_gran((GAS u64*)(wsg + WS_FH + (((b * 32 + dst) * 32 + j32) * 16 + ci) * 8), tag, __float_as_uint(s));
      }
    }
    STP(15);
    WAITV(0); SB(); ldk(7, K7); SB();
    STP(1);
    BAR();
    STP(2);
    const int c = lane & 31;
    const v4u q0 = *(const LAS v4u*)(lds + L_QV + c * 16), q1 = *(const LAS v4u*)(lds + L_QV + 512 + c * 16);
    LAS float* scw = (LAS float*)(lds + L_SC) + w * SC_STRIDE;
    float mrun = -INFINITY;
    const int hsel = (lane >> 2) & 1, ksel = 2 * ((lane >> 3) & 3) + (lane >> 5);
    auto kcomp = [&](int i, const v4u (&r)[4]) {
      float xx[8];
#pragma unroll
      for (int u = 0; u < 4; ++u) {
        float a = 0.f, bb = 0.f;
#pragma unroll
        for (int k = 0; k < 4; ++k) { a = dot2(r[u][k], q0[k], a); bb = dot2(r[u][k], q1[k], bb); }
        xx[2 * u] = a; xx[2 * u + 1] = bb;
      }
      float y[4];
#pragma unroll
      for (int v = 0; v < 4; ++v) {
        auto s = __builtin_amdgcn_permlane16_swap(__float_as_uint(xx[v]), __float_as_uint(xx[v + 4]), false, false);
        y[v] = __uint_as_float(s[0]) + __uint_as_float(s[1]);
      }
      float z[2];
#pragma unroll
      for (int v = 0; v < 2; ++v) {
        const float t1 = y[v] + dpp<ROR8>(y[v]), t2 = y[v + 2] + dpp<ROR8>(y[v + 2]);
        z[v] = (lane & 8) ? t2 : t1;
      }
      const float t1 = z[0] + dpp<HMIR>(z[0]), t2 = z[1] + dpp<HMIR>(z[1]);
      float s = (lane & 4) ? t2 : t1;
      s += dpp<XOR2>(s); s += dpp<XOR1>(s);
      s = rbf(s) * 0.0625f;
      mrun = fmaxf(mrun, s);
      if ((lane & 3) == 0) scw[hsel * 256 + i * 8 + ksel] = s;
    };
    kcomp(0, K0); SB(); kcomp(1, K1); SB(); kcomp(2, K2); SB(); kcomp(3, K3); SB(); kcomp(4, K4); SB(); kcomp(5, K5); SB(); kcomp(6, K6); SB();
#pragma unroll 1
    for (int i = 7; i < NBATCH - 1; i += 2) {
      WAITV(0); SB();
      ldk(i + 1, K0); SB();
      kcomp(i, K7); SB();
      WAITV(0); SB();
      ldk(i + 2, K7); SB();
      kcomp(i + 1, K0); SB();
    }
    WAITV(0); SB();
    ldv(0, K0); SB();
    kcomp(NBATCH - 1, K7); SB();
    STP(3);
    mrun = fmaxf(mrun, dpp<ROR8>(mrun)); mrun = max16(mrun); mrun = max32(mrun);
    const float m0 = __builtin_bit_cast(float, __builtin_amdgcn_readlane(__builtin_bit_cast(int, mrun), 0));
    const float m1 = __builtin_bit_cast(float, __builtin_amdgcn_readlane(__builtin_bit_cast(int, mrun), 4));
    const int hh = lane >> 5;
    const float mh = hh ? m1 : m0;
    float lsum;
    {
      WAITL();
      const f32x4 s0 = *(LAS f32x4*)(scw + hh * 256 + c * 8), s1 = *(LAS f32x4*)(scw + hh * 256 + c * 8 + 4);
      WAITL();
      unsigned pw[8]; lsum = 0.f;
#pragma unroll
      for (int e = 0; e < 4; ++e) {
        const float p0 = __builtin_amdgcn_exp2f((s0[e] - mh) * L2E), p1 = __builtin_amdgcn_exp2f((s1[e] - mh) * L2E);
        lsum += p0 + p1;
        pw[e] = pk2(p0, p0); pw[e + 4] = pk2(p1, p1);
      }
      LAS unsigned* pwl = (LAS unsigned*)scw;
#pragma unroll
      for (int g = 0; g < 4; ++g) *(LAS v2u*)(pwl + (hh * 4 + g) * 68 + 2 * c) = v2u{pw[g], pw[g + 4]};
      lsum += dpp<XOR1>(lsum); lsum += dpp<XOR2>(lsum); lsum += dpp<HMIR>(lsum); lsum += dpp<ROR8>(lsum); lsum = sum16(lsum);
      WAITL();
    }
    f32x4 acc0 = {0, 0, 0, 0}, acc1 = {0, 0, 0, 0};
    const int ii = lane & 15, g = lane >> 4, hV = ii >> 3, jj = ii & 7;
    unsigned msk[4];
#pragma unroll
    for (int r = 0; r < 4; ++r) msk[r] = (r == (jj >> 1)) ? ((jj & 1) ? 0xffff0000u : 0x0000ffffu) : 0u;
    const LAS unsigned* prd = (const LAS unsigned*)scw + (hV * 4 + g) * 68;
    auto vcomp = [&](int i, const v4u (&r)[4]) {
      const v2u pw = *(const LAS v2u*)(prd + 2 * i);
#pragma unroll
      for (int rg = 0; rg < 2; ++rg) {
        const unsigned pv = pw[rg];
        const v4u a = {pv & msk[0], pv & msk[1], pv & msk[2], pv & msk[3]};
        acc0 = mfma(a, r[2 * rg], acc0);
        acc1 = mfma(a, r[2 * rg + 1], acc1);
      }
    };
#pragma unroll 1
    for (int i = 0; i < NBATCH; i += 2) {
      WAITV(0); SB();
      ldv(i + 1, K7); SB();
      vcomp(i, K0); SB();
      WAITV(0); SB();
      if (i + 2 < NBATCH) ldv(i + 2, K0);
      SB();
      vcomp(i + 1, K7); SB();
    }
    STP(4);
    LAS float* accw = (LAS float*)(lds + L_SC) + w * SC_STRIDE + hh * 256 + 8 * (lane & 15) + 4 * ((lane >> 4) & 1);
    *(LAS f32x4*)(accw) = acc0; *(LAS f32x4*)(accw + 128) = acc1;
    LAS float* ml = (LAS float*)(lds + L_ML) + w * 4;
    if (c == 0) { ml[hh * 2] = mh; ml[hh * 2 + 1] = lsum; }
  } else if (w == 8) {
    const unsigned E = __hip_atomic_load((GAS unsigned*)(wsg + WS_EPOCH), RLX_AGENT);
    const int pidx = sp * 8 + b;
    {
      const u16* src = wq + (size_t)(lane >> 1) * NQ + kvh * 512 + pidx * 16 + (lane & 1) * 8;
#pragma unroll
      for (int it = 0; it < 14; ++it)
        __builtin_amdgcn_global_load_lds((const unsigned*)(src + (size_t)it * 32 * NQ), (unsigned*)(lds_ + L_WQ + it * 1024), 16, 0, AUXPOL);
    }
    unsigned warm;
    {
      const GAS unsigned char* p;
      if (lane < 16) p = (const GAS unsigned char*)vc + kvbase + (size_t)lane * 65536;
      else if (lane < 32) p = (const GAS unsigned char*)kc + kvbase + (size_t)(lane - 16) * 65536;
      else if (lane < 40) p = wsg + WS_PBOX + (size_t)(b * 8 + kvh) * 16 * PMSG + (lane - 32) * 2176;
      else if (lane < 48) p = wsg + WS_FS + (size_t)b * 131072 + (lane - 40) * 16384;
      else if (lane < 56) p = wsg + WS_FH + (size_t)b * 131072 + (lane - 48) * 16384;
      else p = (const GAS unsigned char*)wo + ((size_t)(kvh * 512 + sp * 128)) * 896 + (lane - 56) * 14336;
      warm = __builtin_nontemporal_load((const GAS unsigned*)p);
    }
    tag = E * 0x9E3779B1u + 0x7F4A7C15u;
    if (lane < 4) ((LAS unsigned*)(lds + L_CNT))[lane] = 0u;
    if (lane == 0) *(LAS unsigned*)(lds + L_TAG) = tag;
    BAR();
    WAITV(1);
    STP(10);
    {
      const int i16 = lane & 15, g = lane >> 4;
      const unsigned aaddr = L_XN + (i16 & 7) * 896 + g * 16;
      const unsigned baddr = (unsigned)(size_t)(lds) + L_WQ + (8 * g + (i16 >> 2)) * 32 + (i16 & 3) * 8;
      v4u a[14]; v2u b0[14], b1v[14];
#pragma unroll
      for (int ks = 0; ks < 14; ++ks) a[ks] = *(const LAS v4u*)(lds + aaddr + ks * 64);
#pragma unroll
      for (int ks = 0; ks < 14; ++ks)
        asm volatile("ds_read_b64_tr_b16 %0, %2 offset:%3\n\tds_read_b64_tr_b16 %1, %2 offset:%4" : "=&v"(b0[ks]), "=&v"(b1v[ks]) : "v"(baddr), "i"(ks * 1024), "i"(ks * 1024 + 128) : "memory");
      asm volatile("s_waitcnt lgkmcnt(0)" : "+v"(b0[0]), "+v"(b0[1]), "+v"(b0[2]), "+v"(b0[3]), "+v"(b0[4]), "+v"(b0[5]), "+v"(b0[6]), "+v"(b0[7]), "+v"(b0[8]), "+v"(b0[9]), "+v"(b0[10]), "+v"(b0[11]), "+v"(b0[12]), "+v"(b0[13]) :: "memory");
      asm volatile("" : "+v"(b1v[0]), "+v"(b1v[1]), "+v"(b1v[2]), "+v"(b1v[3]), "+v"(b1v[4]), "+v"(b1v[5]), "+v"(b1v[6]), "+v"(b1v[7]), "+v"(b1v[8]), "+v"(b1v[9]), "+v"(b1v[10]), "+v"(b1v[11]), "+v"(b1v[12]), "+v"(b1v[13]) :: "memory");
      SB();
      f32x4 dq0 = {0, 0, 0, 0}, dq1 = {0, 0, 0, 0};
#pragma unroll
      for (int ks = 0; ks < 14; ks += 2) {
        dq0 = mfma(a[ks], v4u{b0[ks][0], b0[ks][1], b1v[ks][0], b1v[ks][1]}, dq0);
        dq1 = mfma(a[ks + 1], v4u{b0[ks + 1][0], b0[ks + 1][1], b1v[ks + 1][0], b1v[ks + 1][1]}, dq1);
      }
      const f32x4 dq = dq0 + dq1;
      LAS u16* qs = (LAS u16*)(lds + L_QS);
      if (g < 2) {
#pragma unroll
        for (int r = 0; r < 4; ++r) qs[(4 * g + r) * 16 + i16] = (u16)(pk2(dq[r], dq[r]) & 0xffffu);
      }
      WAITL();
      const unsigned qw = *(const LAS unsigned*)(lds + L_QS + lane * 4);
      st_gran((GAS u64*)(wsg + WS_QBOX + (((kvh * 8 + (lane >> 3)) * 32 + pidx) * 8 + (lane & 7)) * 8), tag, qw);
      if (lane == 0) ((volatile LAS unsigned*)(lds + L_CNT))[2] = 1u;
    }
    STP(1);
    {
      const __amdgpu_buffer_rsrc_t rq = __builtin_amdgcn_make_buffer_rsrc((void*)(ws + WS_QBOX + (kvh * 8 + b) * 2048), 0, 2048, 0x00020000);
      v4u g0, g1; int npoll = 0;
      for (;;) {
        asm volatile("" ::: "memory");
        ++npoll;
        __builtin_amdgcn_s_sleep(POLLSLEEP);
        g0 = __builtin_amdgcn_raw_buffer_load_b128(rq, lane * 32, 0, 16);
        g1 = __builtin_amdgcn_raw_buffer_load_b128(rq, lane * 32 + 16, 0, 16);
        const bool ok = g0[1] == tag && g0[3] == tag && g1[1] == tag && g1[3] == tag;
        if (__all(ok)) break;
        if (rt() - tstart > TMO) break;
      }
      *(LAS v4u*)(lds + L_QV + lane * 16) = v4u{g0[0], g0[2], g1[0], g1[2]};
    }
    STP(2);
    BAR();
    asm volatile("" :: "v"(warm));
    {
      GAS u64* fb = (GAS u64*)(wsg + WS_FH + (b * 32 + j32) * 4096) + lane;
      float s = 0.f;
      for (;;) {
        __builtin_amdgcn_s_sleep(64);
        asm volatile("" ::: "memory");
        bool ok = true; s = 0.f;
        u64 gv[8];
#pragma unroll
        for (int k = 0; k < 8; ++k) gv[k] = ld_gran(fb + k * 64);
#pragma unroll
        for (int k = 0; k < 8; ++k) { ok &= ((unsigned)(gv[k] >> 32) == tag) || ((lane & 15) >= 14); s += __uint_as_float((unsigned)gv[k]); }
        if (__all(ok)) break;
        if (rt() - tstart > TMO) break;
      }
      s = sum16(s); s = sum32(s);
      if (lane < 14) {
        const int col = j32 * 14 + lane;
        const float gg = rbf(rbf(s) + bf2f(b2[col]));
        const float sg = rbf(gg / (1.0f + __builtin_amdgcn_exp2f(-gg * L2E)));
        LAS float* gl = (LAS float*)(lds + L_G);
        gl[lane] = sg;
        gl[16 + lane] = bf2f(x[b * D + col]);
      }
    }
    STP(5);
  } else {
    {
      const int half = w - 9;
      const u16* src = wo + ((size_t)(kvh * 512 + sp * 128)) * D + half * 56 * 512 + lane * 8;
      __builtin_amdgcn_s_sleep(WDELAY);
#pragma unroll
      for (int k = 0; k < (ABL_WO ? 0 : 56); ++k)
        __builtin_amdgcn_global_load_lds((const unsigned*)(src + (size_t)k * 512), (unsigned*)(lds_ + L_WO + (half * 56 + k) * 1024), 16, 0, AUXPOL);
      BAR();
      BAR();
      WAITV(0);
      if (w == 9) {
        while (rt() - tstart < CODEPF) __builtin_amdgcn_s_sleep(64);
        unsigned lo, hi, d0, len;
        asm volatile("s_getpc_b64 s[40:41]\n"
                     "fused_pc_here:\n\t"
                     "s_mov_b32 %0, s40\n\t"
                     "s_mov_b32 %1, s41\n\t"
                     "s_mov_b32 %2, fused_pc_here-fused_code_begin\n\t"
                     "s_mov_b32 %3, fused_code_end-fused_code_begin"
                     : "=s"(lo), "=s"(hi), "=s"(d0), "=s"(len) :: "s40", "s41");
        const u64 begin = (((u64)hi << 32) | lo) - d0;
        const unsigned off = (unsigned)((bid >> 3) & 31) * 1024u + lane * 16u;
        if (off + 16 <= len) { const v4u cv = *(const GAS v4u*)(begin + off); asm volatile("" :: "v"(cv)); }
      }
    }
  }
  BAR();
  const int pair = b * 8 + kvh;
  if (w < 8) {
    const int h = tid >> 8;
    const LAS float* ml = (const LAS float*)(lds + L_ML);
    float M = -INFINITY;
#pragma unroll
    for (int ww = 0; ww < 8; ++ww) M = fmaxf(M, ml[ww * 4 + h * 2]);
    float L = 0.f, o = 0.f;
    const LAS float* ac = (const LAS float*)(lds + L_SC) + tid;
#pragma unroll
    for (int ww = 0; ww < 8; ++ww) { const float sc = __builtin_amdgcn_exp2f((ml[ww * 4 + h * 2] - M) * L2E); L += sc * ml[ww * 4 + h * 2 + 1]; o += sc * ac[ww * SC_STRIDE]; }
    const int dst = tid >> 7, idx = tid & 127;
    if (dst != sp) {
      GAS u64* pb = (GAS u64*)(wsg + WS_PBOX + ((pair * 4 + dst) * 4 + sp) * PMSG);
      st_gran(pb + idx, tag, __float_as_uint(o));
      if (idx == 0) { st_gran(pb + 128, tag, __float_as_uint(M)); st_gran(pb + 129, tag, __float_as_uint(L)); }
    } else {
      GAS u64* pb = (GAS u64*)(wsg + WS_PBOX + ((pair * 4 + sp) * 4) * PMSG);
      float ov[3], mv[3], lv[3];
      for (;;) {
        asm volatile("" ::: "memory");
        u64 gv[3], gm[3];
#pragma unroll
        for (int k = 0; k < 3; ++k) {
          const int src = (sp + 1 + k) & 3;
          gv[k] = ld_gran(pb + src * (PMSG / 8) + idx);
          gm[k] = ld_gran(pb + src * (PMSG / 8) + 128 + (lane & 1));
        }
        bool ok = true;
#pragma unroll
        for (int k = 0; k < 3; ++k) {
          ok &= ((unsigned)(gv[k] >> 32) == tag) && ((unsigned)(gm[k] >> 32) == tag);
          ov[k] = __uint_as_float((unsigned)gv[k]);
          mv[k] = __builtin_bit_cast(float, __builtin_amdgcn_readlane((int)(unsigned)gm[k], 0));
          lv[k] = __builtin_bit_cast(float, __builtin_amdgcn_readlane((int)(unsigned)gm[k], 1));
        }
        if (__all(ok)) break;
        if (rt() - tstart > TMO) break;
      }
      const float Mx = fmaxf(fmaxf(M, mv[0]), fmaxf(mv[1], mv[2]));
      const float eo = __builtin_amdgcn_exp2f((M - Mx) * L2E), e0 = __builtin_amdgcn_exp2f((mv[0] - Mx) * L2E), e1 = __builtin_amdgcn_exp2f((mv[1] - Mx) * L2E), e2 = __builtin_amdgcn_exp2f((mv[2] - Mx) * L2E);
      const float Lt = eo * L + e0 * lv[0] + e1 * lv[1] + e2 * lv[2];
      const float of = (eo * o + e0 * ov[0] + e1 * ov[1] + e2 * ov[2]) / Lt;
      ((LAS float*)(lds + L_OS))[idx] = rbf(of);
    }
  }
  STP(w < 8 ? 5 : 6);
  BAR();
  if (w < 8) {
    const int rg = lane & 7, cc = (lane >> 3) < 7 ? (lane >> 3) : 6;
    float a[8] = {0, 0, 0, 0, 0, 0, 0, 0};
    const LAS float* os = (const LAS float*)(lds + L_OS) + rg;
    const LAS unsigned char* wp = lds + L_WO + rg * 896 + (7 * w + cc) * 16;
#pragma unroll
    for (int r = 0; r < 16; ++r) {
      const v4u wv = *(const LAS v4u*)(wp + r * 8 * 896);
      const float ov = os[8 * r];
#pragma unroll
      for (int e = 0; e < 4; ++e) { a[2 * e] += ov * bflo(wv[e]); a[2 * e + 1] += ov * bfhi(wv[e]); }
    }
    float y[4];
#pragma unroll
    for (int v = 0; v < 4; ++v) { const float t1 = a[v] + dpp<HMIR>(a[v]), t2 = a[v + 4] + dpp<HMIR>(a[v + 4]); y[v] = (lane & 4) ? t2 : t1; }
    float z[2];
#pragma unroll
    for (int v = 0; v < 2; ++v) { const float t1 = y[v] + dpp<XOR2>(y[v]), t2 = y[v + 2] + dpp<XOR2>(y[v + 2]); z[v] = (lane & 2) ? t2 : t1; }
    const float t1 = z[0] + dpp<XOR1>(z[0]), t2 = z[1] + dpp<XOR1>(z[1]);
    const float s = (lane & 1) ? t2 : t1;
    if (lane < 56) {
      const int col = 56 * w + lane;
      const int dst = col / 14, ci = col - dst * 14;
      st_gran((GAS u64*)(wsg + WS_FS + (((b * 32 + dst) * 32 + j32) * 16 + ci) * 8), tag, __float_as_uint(s));
    }
  }
  if (w == 0) {
    GAS u64* fb = (GAS u64*)(wsg + WS_FS + (b * 32 + j32) * 4096) + lane;
    float s;
    for (;;) {
      asm volatile("" ::: "memory");
      u64 gv[8]; bool ok = true; s = 0.f;
#pragma unroll
      for (int k = 0; k < 8; ++k) gv[k] = ld_gran(fb + k * 64);
#pragma unroll
      for (int k = 0; k < 8; ++k) { ok &= ((unsigned)(gv[k] >> 32) == tag) || ((lane & 15) >= 14); s += __uint_as_float((unsigned)gv[k]); }
      if (__all(ok)) break;
      if (rt() - tstart > TMO) break;
    }
    s = sum16(s); s = sum32(s);
    if (lane < 14) {
      const int col = j32 * 14 + lane;
      const float sg = ((const LAS float*)(lds + L_G))[lane], xv = ((const LAS float*)(lds + L_G))[16 + lane];
      const float r = xv + rbf(rbf(s) * sg);
      out[b * D + col] = (u16)(pk2(r, r) & 0xffffu);
    }
    if (bid == 0 && lane == 0) {
      const unsigned E = __hip_atomic_load((GAS unsigned*)(wsg + WS_EPOCH), RLX_AGENT);
      __hip_atomic_store((GAS unsigned*)(wsg + WS_EPOCH), E + 1u, RLX_AGENT);
    }
  }
  asm volatile("fused_code_end:");
  STP(7);
}

extern "C" void kernel_launch(void* const* d_in, const int* in_sizes, int n_in, void* d_out, int out_size, void* d_ws, size_t ws_size, hipStream_t stream) {
  static bool attr_set = false;
  if (!attr_set) { (void)hipFuncSetAttribute((const void*)fused, hipFuncAttributeMaxDynamicSharedMemorySize, L_END); attr_set = true; }
  hipLaunchKernelGGL(fused, dim3(256), dim3(NTHREADS), L_END, stream,
                     (const u16*)d_in[0], (const u16*)d_in[1], (const u16*)d_in[2], (const u16*)d_in[3], (const u16*)d_in[4],
                     (const u16*)d_in[6], (const u16*)d_in[7], (const u16*)d_in[8], (const u16*)d_in[9], (u16*)d_out, (unsigned char*)d_ws);
}
```

```cpp
#include <hip/hip_runtime.h>
#include <hip/hip_bf16.h>
#include <stdint.h>

typedef unsigned short u16;
typedef unsigned v4u __attribute__((ext_vector_type(4)));
typedef unsigned v2u __attribute__((ext_vector_type(2)));
typedef float f32x4 __attribute__((ext_vector_type(4)));
typedef short bf16x8 __attribute__((ext_vector_type(8)));
typedef __bf16 bf16x2 __attribute__((ext_vector_type(2)));
typedef unsigned long long u64;
#define LAS __attribute__((address_space(3)))
#define GAS __attribute__((address_space(1)))
#define RLX_AGENT __ATOMIC_RELAXED, __HIP_MEMORY_SCOPE_AGENT

constexpr int D = 448, NQ = 4096, SK = 8192, F = 1792;
constexpr int AUXPOL = 0;
constexpr int NPRE = 8;
constexpr int WOB = 28;
constexpr int NBATCH = 32;
constexpr float L2E = 1.4426950408889634f;
constexpr int NTHREADS = 640;

constexpr unsigned WS_EPOCH = 0;
constexpr unsigned WS_QBOX = 4096;
constexpr unsigned WS_PBOX = WS_QBOX + 8 * 8 * 32 * 64;
constexpr unsigned PMSG = 136 * 8;
constexpr unsigned WS_FS = WS_PBOX + 64 * 16 * PMSG;
constexpr unsigned WS_FH = WS_FS + 8 * 32 * 32 * 128;
constexpr unsigned WS_END = WS_FH + 8 * 32 * 32 * 128;

constexpr int L_WO = 0;
constexpr int SC_STRIDE = 8 * 68;
constexpr int L_SC = L_WO + 128 * 896;
constexpr int L_XN = L_SC + 8 * SC_STRIDE * 4;
constexpr int L_WQ = L_XN + 8 * 896;
constexpr int L_QV = L_WQ + 448 * 32;
constexpr int L_TAG = L_QV + 1024, L_QS = L_TAG + 16, L_OS = L_QS + 256, L_G = L_OS + 512, L_HRED = L_G + 128, L_HS = L_HRED + 2016, L_ML = L_HS + 224;
constexpr int L_END = L_ML + 128;
static_assert(L_END <= 160 * 1024, "lds");

__device__ __forceinline__ float bf2f(u16 b) { return __uint_as_float(((unsigned)b) << 16); }
__device__ __forceinline__ float bflo(unsigned w) { return __uint_as_float(w << 16); }
__device__ __forceinline__ float bfhi(unsigned w) { return __uint_as_float(w & 0xffff0000u); }
__device__ __forceinline__ unsigned pk2(float a, float b) { bf16x2 v = {(__bf16)a, (__bf16)b}; return __builtin_bit_cast(unsigned, v); }
__device__ __forceinline__ float rbf(float f) { return bflo(pk2(f, f)); }
__device__ __forceinline__ float dot2(unsigned k, unsigned q, float acc) {
  return __builtin_amdgcn_fdot2_f32_bf16(__builtin_bit_cast(bf16x2, k), __builtin_bit_cast(bf16x2, q), acc, false);
}
template <int CTRL> __device__ __forceinline__ float dpp(float x) {
  return __builtin_bit_cast(float, __builtin_amdgcn_mov_dpp(__builtin_bit_cast(int, x), CTRL, 0xf, 0xf, true));
}
constexpr int XOR1 = 0xB1, XOR2 = 0x4E, HMIR = 0x141, ROR8 = 0x128;
__device__ __forceinline__ float sum16(float x) {
  auto s = __builtin_amdgcn_permlane16_swap(__float_as_uint(x), __float_as_uint(x), false, false); return __uint_as_float(s[0]) + __uint_as_float(s[1]); }
__device__ __forceinline__ float sum32(float x) {
  auto s = __builtin_amdgcn_permlane32_swap(__float_as_uint(x), __float_as_uint(x), false, false); return __uint_as_float(s[0]) + __uint_as_float(s[1]); }
__device__ __forceinline__ float max16(float x) {
  auto s = __builtin_amdgcn_permlane16_swap(__float_as_uint(x), __float_as_uint(x), false, false); return fmaxf(__uint_as_float(s[0]), __uint_as_float(s[1])); }
__device__ __forceinline__ float max32(float x) {
  auto s = __builtin_amdgcn_permlane32_swap(__float_as_uint(x), __float_as_uint(x), false, false); return fmaxf(__uint_as_float(s[0]), __uint_as_float(s[1])); }
__device__ __forceinline__ float wsum64(float x) {
  x += dpp<XOR1>(x); x += dpp<XOR2>(x); x += dpp<HMIR>(x); x += dpp<ROR8>(x); x = sum16(x); return sum32(x); }
__device__ __forceinline__ f32x4 mfma(v4u a, v4u b, f32x4 c) {
  return __builtin_amdgcn_mfma_f32_16x16x32_bf16(__builtin_bit_cast(bf16x8, a), __builtin_bit_cast(bf16x8, b), c, 0, 0, 0);
}
__device__ __forceinline__ u64 rt() { return __builtin_amdgcn_s_memrealtime(); }
#define WAITV(n) asm volatile("s_waitcnt vmcnt(" #n ")" ::: "memory")
#define WAITL() asm volatile("s_waitcnt lgkmcnt(0)" ::: "memory")
#define SB() __builtin_amdgcn_sched_barrier(0)
#define ISSUE_GAP() __builtin_amdgcn_s_sleep(5)
#define BAR() do { WAITL(); __builtin_amdgcn_s_barrier(); asm volatile("" ::: "memory"); } while (0)
__device__ __forceinline__ void st_gran(GAS u64* p, unsigned tag, unsigned val) { __hip_atomic_store(p, ((u64)tag << 32) | val, RLX_AGENT); }
__device__ __forceinline__ u64 ld_gran(GAS u64* p) { return __hip_atomic_load(p, RLX_AGENT); }
constexpr u64 TMO = 400000;
constexpr unsigned OOB = 0x7fffff00u;


__global__ __launch_bounds__(NTHREADS) void fused(const u16* __restrict__ x, const u16* __restrict__ kc, const u16* __restrict__ vc, const u16* __restrict__ wq, const u16* __restrict__ wo,
                                             const u16* __restrict__ w1, const u16* __restrict__ b1, const u16* __restrict__ w2, const u16* __restrict__ b2, u16* __restrict__ out, unsigned char* __restrict__ ws) {
  extern __shared__ __attribute__((aligned(16))) unsigned char lds_[];
  LAS unsigned char* lds = (LAS unsigned char*)lds_;
  const int bid = blockIdx.x; const int b = bid >> 5, j32 = bid & 31, kvh = j32 >> 2, sp = bid & 3;
  const int tid = threadIdx.x, lane = tid & 63; const int w = __builtin_amdgcn_readfirstlane(tid >> 6);
  GAS unsigned char* wsg = (GAS unsigned char*)ws;
  const u64 tstart = rt();
  unsigned tag = 0;
  const size_t kvbase = ((size_t)(b * 8 + kvh) * SK + (size_t)sp * 2048) * 512;

  if (w < 8) {
    const __amdgpu_buffer_rsrc_t rk = __builtin_amdgcn_make_buffer_rsrc((void*)((const char*)kc + kvbase), 0, 1u << 20, 0x00020000);
    const __amdgpu_buffer_rsrc_t rv = __builtin_amdgcn_make_buffer_rsrc((void*)((const char*)vc + kvbase), 0, 1u << 20, 0x00020000);
    const __amdgpu_buffer_rsrc_t rx = __builtin_amdgcn_make_buffer_rsrc((void*)x, 0, 8 * 896, 0x00020000);
    const unsigned koff = lane * 16;
    const unsigned voff = (lane >> 4) * 512 + (lane & 15) * 16;
    auto ldk = [&](int i, v4u (&r)[4]) {
#pragma unroll
      for (int u = 0; u < 4; ++u) r[u] = __builtin_amdgcn_raw_buffer_load_b128(rk, koff + u * 1024, (unsigned)(i * 8 + w) * 4096u, 2);
    };
    auto ldv = [&](int i, v4u (&r)[4]) {
#pragma unroll
      for (int u = 0; u < 4; ++u) r[u] = __builtin_amdgcn_raw_buffer_load_b128(rv, voff + (u >> 1) * 2048 + (u & 1) * 256, (unsigned)(i * 8 + w) * 4096u, 2);
    };
    v4u KB[NPRE][4];
    const v4u xr = __builtin_amdgcn_raw_buffer_load_b128(rx, lane < 56 ? (unsigned)(w * 896 + lane * 16) : OOB, 0, 0);
    SB();
    ldk(0, KB[0]); SB();
    {
      float v[8];
#pragma unroll
      for (int k = 0; k < 4; ++k) { v[2 * k] = bflo(xr[k]); v[2 * k + 1] = bfhi(xr[k]); }
      float s = ((v[0] + v[1]) + (v[2] + v[3])) + ((v[4] + v[5]) + (v[6] + v[7]));
      s = wsum64(s);
      const float mean = s * (1.0f / 448.0f);
      float q = 0.f;
#pragma unroll
      for (int k = 0; k < 8; ++k) { const float d = v[k] - mean; q += d * d; }
      q = lane < 56 ? q : 0.f;
      q = wsum64(q);
      const float meanb = rbf(mean);
      const float den = rbf(sqrtf(rbf(rbf(q * (1.0f / 448.0f)) + 1e-5f)));
      float rc = __builtin_amdgcn_rcpf(den); rc = rc * (2.0f - den * rc);
      v4u o;
#pragma unroll
      for (int k = 0; k < 4; ++k) { const unsigned d2 = pk2(v[2 * k] - meanb, v[2 * k + 1] - meanb); o[k] = pk2(bflo(d2) * rc, bfhi(d2) * rc); }
      if (lane < 56) *(LAS v4u*)(lds + L_XN + w * 896 + lane * 16) = o;
    }
    BAR();
#pragma unroll
    for (int p = 1; p < NPRE; ++p) { WAITV(0); SB(); ldk(p, KB[p]); SB(); }
    BAR();
    const int c = lane & 31;
    const v4u q0 = *(const LAS v4u*)(lds + L_QV + c * 16), q1 = *(const LAS v4u*)(lds + L_QV + 512 + c * 16);
    tag = *(const LAS unsigned*)(lds + L_TAG);
    LAS float* scw = (LAS float*)(lds + L_SC) + w * SC_STRIDE;
    float mrun = -INFINITY;
    const int hsel = (lane >> 2) & 1, ksel = 2 * ((lane >> 3) & 3) + (lane >> 5);
    auto kcomp = [&](int i, const v4u (&r)[4]) {
      float xx[8];
#pragma unroll
      for (int u = 0; u < 4; ++u) {
        float a = 0.f, bb = 0.f;
#pragma unroll
        for (int k = 0; k < 4; ++k) { a = dot2(r[u][k], q0[k], a); bb = dot2(r[u][k], q1[k], bb); }
        xx[2 * u] = a; xx[2 * u + 1] = bb;
      }
      float y[4];
#pragma unroll
      for (int v = 0; v < 4; ++v) {
        auto s = __builtin_amdgcn_permlane16_swap(__float_as_uint(xx[v]), __float_as_uint(xx[v + 4]), false, false);
        y[v] = __uint_as_float(s[0]) + __uint_as_float(s[1]);
      }
      float z[2];
#pragma unroll
      for (int v = 0; v < 2; ++v) {
        const float t1 = y[v] + dpp<ROR8>(y[v]), t2 = y[v + 2] + dpp<ROR8>(y[v + 2]);
        z[v] = (lane & 8) ? t2 : t1;
      }
      const float t1 = z[0] + dpp<HMIR>(z[0]), t2 = z[1] + dpp<HMIR>(z[1]);
      float s = (lane & 4) ? t2 : t1;
      s += dpp<XOR2>(s); s += dpp<XOR1>(s);
      s = rbf(s) * 0.0625f;
      mrun = fmaxf(mrun, s);
      if ((lane & 3) == 0) scw[hsel * 256 + i * 8 + ksel] = s;
    };
#pragma unroll
    for (int p = 0; p < NPRE - 1; ++p) { kcomp(p, KB[p]); SB(); }
#pragma unroll 1
    for (int i = NPRE - 1; i < NBATCH - 1; i += 2) {
      WAITV(0); SB(); ISSUE_GAP();
      ldk(i + 1, KB[0]); SB();
      kcomp(i, KB[NPRE - 1]); SB();
      WAITV(0); SB(); ISSUE_GAP();
      ldk(i + 2, KB[NPRE - 1]); SB();
      kcomp(i + 1, KB[0]); SB();
    }
    WAITV(0); SB();
    ldv(0, KB[0]); SB();
    kcomp(NBATCH - 1, KB[NPRE - 1]); SB();
    if (w == 0 && lane == 0) *(volatile LAS unsigned*)(lds + L_TAG + 4) = 1u;
    mrun = fmaxf(mrun, dpp<ROR8>(mrun)); mrun = max16(mrun); mrun = max32(mrun);
    const float m0 = __builtin_bit_cast(float, __builtin_amdgcn_readlane(__builtin_bit_cast(int, mrun), 0));
    const float m1 = __builtin_bit_cast(float, __builtin_amdgcn_readlane(__builtin_bit_cast(int, mrun), 4));
    const int hh = lane >> 5;
    const float mh = hh ? m1 : m0;
    float lsum;
    {
      WAITL();
      const f32x4 s0 = *(LAS f32x4*)(scw + hh * 256 + c * 8), s1 = *(LAS f32x4*)(scw + hh * 256 + c * 8 + 4);
      WAITL();
      unsigned pw[8]; lsum = 0.f;
#pragma unroll
      for (int e = 0; e < 4; ++e) {
        const float p0 = __builtin_amdgcn_exp2f((s0[e] - mh) * L2E), p1 = __builtin_amdgcn_exp2f((s1[e] - mh) * L2E);
        lsum += p0 + p1;
        pw[e] = pk2(p0, p0); pw[e + 4] = pk2(p1, p1);
      }
      LAS unsigned* pwl = (LAS unsigned*)scw;
#pragma unroll
      for (int g = 0; g < 4; ++g) *(LAS v2u*)(pwl + (hh * 4 + g) * 68 + 2 * c) = v2u{pw[g], pw[g + 4]};
      lsum += dpp<XOR1>(lsum); lsum += dpp<XOR2>(lsum); lsum += dpp<HMIR>(lsum); lsum += dpp<ROR8>(lsum); lsum = sum16(lsum);
      WAITL();
    }
    f32x4 acc0 = {0, 0, 0, 0}, acc1 = {0, 0, 0, 0};
    const int ii = lane & 15, g = lane >> 4, hV = ii >> 3, jj = ii & 7;
    unsigned msk[4];
#pragma unroll
    for (int r = 0; r < 4; ++r) msk[r] = (r == (jj >> 1)) ? ((jj & 1) ? 0xffff0000u : 0x0000ffffu) : 0u;
    const LAS unsigned* prd = (const LAS unsigned*)scw + (hV * 4 + g) * 68;
    auto vcomp = [&](int i, const v4u (&r)[4]) {
      const v2u pw = *(const LAS v2u*)(prd + 2 * i);
#pragma unroll
      for (int rg = 0; rg < 2; ++rg) {
        const unsigned pv = pw[rg];
        const v4u a = {pv & msk[0], pv & msk[1], pv & msk[2], pv & msk[3]};
        acc0 = mfma(a, r[2 * rg], acc0);
        acc1 = mfma(a, r[2 * rg + 1], acc1);
      }
    };
#pragma unroll 1
    for (int i = 0; i < NBATCH; i += 2) {
      WAITV(0); SB(); ISSUE_GAP();
      ldv(i + 1, KB[NPRE - 1]); SB();
      vcomp(i, KB[0]); SB();
      WAITV(0); SB(); ISSUE_GAP();
      if (i + 2 < NBATCH) ldv(i + 2, KB[0]);
      SB();
      vcomp(i + 1, KB[NPRE - 1]); SB();
    }
    LAS float* accw = (LAS float*)(lds + L_SC) + w * SC_STRIDE + hh * 256 + 8 * (lane & 15) + 4 * ((lane >> 4) & 1);
    *(LAS f32x4*)(accw) = acc0; *(LAS f32x4*)(accw + 128) = acc1;
    LAS float* ml = (LAS float*)(lds + L_ML) + w * 4;
    if (c == 0) { ml[hh * 2] = mh; ml[hh * 2 + 1] = lsum; }
  } else if (w == 8) {
    const unsigned E = __hip_atomic_load((GAS unsigned*)(wsg + WS_EPOCH), RLX_AGENT);
    const int pidx = sp * 8 + b;
    {
      const u16* src = wq + (size_t)(lane >> 1) * NQ + kvh * 512 + pidx * 16 + (lane & 1) * 8;
#pragma unroll
      for (int it = 0; it < 14; ++it)
        __builtin_amdgcn_global_load_lds((const unsigned*)(src + (size_t)it * 32 * NQ), (unsigned*)(lds_ + L_WQ + it * 1024), 16, 0, AUXPOL);
    }
    tag = E * 0x9E3779B1u + 0x7F4A7C15u;
    if (lane == 0) *(LAS unsigned*)(lds + L_TAG + 4) = 0u;
    BAR();
    WAITV(0);
    {
      const int i16 = lane & 15, g = lane >> 4;
      const unsigned aaddr = L_XN + (i16 & 7) * 896 + g * 16;
      const unsigned baddr = (unsigned)(size_t)(lds) + L_WQ + (8 * g + (i16 >> 2)) * 32 + (i16 & 3) * 8;
      v4u a[14]; v2u b0[14], b1v[14];
#pragma unroll
      for (int ks = 0; ks < 14; ++ks) a[ks] = *(const LAS v4u*)(lds + aaddr + ks * 64);
#pragma unroll
      for (int ks = 0; ks < 14; ++ks)
        asm volatile("ds_read_b64_tr_b16 %0, %2 offset:%3\n\tds_read_b64_tr_b16 %1, %2 offset:%4" : "=&v"(b0[ks]), "=&v"(b1v[ks]) : "v"(baddr), "i"(ks * 1024), "i"(ks * 1024 + 128) : "memory");
      asm volatile("s_waitcnt lgkmcnt(0)" : "+v"(b0[0]), "+v"(b0[1]), "+v"(b0[2]), "+v"(b0[3]), "+v"(b0[4]), "+v"(b0[5]), "+v"(b0[6]), "+v"(b0[7]), "+v"(b0[8]), "+v"(b0[9]), "+v"(b0[10]), "+v"(b0[11]), "+v"(b0[12]), "+v"(b0[13]) :: "memory");
      asm volatile("" : "+v"(b1v[0]), "+v"(b1v[1]), "+v"(b1v[2]), "+v"(b1v[3]), "+v"(b1v[4]), "+v"(b1v[5]), "+v"(b1v[6]), "+v"(b1v[7]), "+v"(b1v[8]), "+v"(b1v[9]), "+v"(b1v[10]), "+v"(b1v[11]), "+v"(b1v[12]), "+v"(b1v[13]) :: "memory");
      SB();
      f32x4 dq0 = {0, 0, 0, 0}, dq1 = {0, 0, 0, 0};
#pragma unroll
      for (int ks = 0; ks < 14; ks += 2) {
        dq0 = mfma(a[ks], v4u{b0[ks][0], b0[ks][1], b1v[ks][0], b1v[ks][1]}, dq0);
        dq1 = mfma(a[ks + 1], v4u{b0[ks + 1][0], b0[ks + 1][1], b1v[ks + 1][0], b1v[ks + 1][1]}, dq1);
      }
      const f32x4 dq = dq0 + dq1;
      LAS u16* qs = (LAS u16*)(lds + L_QS);
      if (g < 2) {
#pragma unroll
        for (int r = 0; r < 4; ++r) qs[(4 * g + r) * 16 + i16] = (u16)(pk2(dq[r], dq[r]) & 0xffffu);
      }
      WAITL();
      const unsigned qw = *(const LAS unsigned*)(lds + L_QS + lane * 4);
      st_gran((GAS u64*)(wsg + WS_QBOX + (((kvh * 8 + (lane >> 3)) * 32 + pidx) * 8 + (lane & 7)) * 8), tag, qw);
    }
    {
      const __amdgpu_buffer_rsrc_t rq = __builtin_amdgcn_make_buffer_rsrc((void*)(ws + WS_QBOX + (kvh * 8 + b) * 2048), 0, 2048, 0x00020000);
      v4u g0, g1;
      for (;;) {
        asm volatile("" ::: "memory");
        __builtin_amdgcn_s_sleep(16);
        g0 = __builtin_amdgcn_raw_buffer_load_b128(rq, lane * 32, 0, 16);
        g1 = __builtin_amdgcn_raw_buffer_load_b128(rq, lane * 32 + 16, 0, 16);
        const bool ok = g0[1] == tag && g0[3] == tag && g1[1] == tag && g1[3] == tag;
        if (__all(ok)) break;
        if (rt() - tstart > TMO) break;
      }
      *(LAS v4u*)(lds + L_QV + lane * 16) = v4u{g0[0], g0[2], g1[0], g1[2]};
      if (lane == 0) *(LAS unsigned*)(lds + L_TAG) = tag;
    }
    BAR();
    unsigned warm;
    {
      const GAS unsigned char* p;
      if (lane < 16) p = (const GAS unsigned char*)vc + kvbase + (size_t)lane * 65536;
      else if (lane < 32) p = (const GAS unsigned char*)kc + kvbase + (size_t)(lane - 16) * 65536;
      else if (lane < 40) p = wsg + WS_PBOX + (size_t)(b * 8 + kvh) * 16 * PMSG + (lane - 32) * 2176;
      else if (lane < 48) p = wsg + WS_FS + (size_t)b * 131072 + (lane - 40) * 16384;
      else if (lane < 56) p = wsg + WS_FH + (size_t)b * 131072 + (lane - 48) * 16384;
      else p = (const GAS unsigned char*)wo + ((size_t)(kvh * 512 + sp * 128)) * 896 + (lane - 56) * 14336;
      warm = __builtin_nontemporal_load((const GAS unsigned*)p);
    }
    asm volatile("" :: "v"(warm));
    {
      GAS u64* fb = (GAS u64*)(wsg + WS_FH + (b * 32 + j32) * 4096) + lane;
      float s = 0.f;
      while (*(volatile LAS unsigned*)(lds + L_TAG + 4) == 0u) __builtin_amdgcn_s_sleep(32);
      for (int it = 0;; ++it) {
        if (it) __builtin_amdgcn_s_sleep(127);
        asm volatile("" ::: "memory");
        bool ok = true; s = 0.f;
        u64 gv[8];
#pragma unroll
        for (int k = 0; k < 8; ++k) gv[k] = ld_gran(fb + k * 64);
#pragma unroll
        for (int k = 0; k < 8; ++k) { ok &= ((unsigned)(gv[k] >> 32) == tag) || ((lane & 15) >= 14); s += __uint_as_float((unsigned)gv[k]); }
        if (__all(ok)) break;
        if (rt() - tstart > TMO) break;
      }
      s = sum16(s); s = sum32(s);
      if (lane < 14) {
        const int col = j32 * 14 + lane;
        const float gg = rbf(rbf(s) + bf2f(b2[col]));
        const float sg = rbf(gg / (1.0f + __builtin_amdgcn_exp2f(-gg * L2E)));
        LAS float* gl = (LAS float*)(lds + L_G);
        gl[lane] = sg;
        gl[16 + lane] = bf2f(x[b * D + col]);
      }
    }
  } else {
    auto wo_dma = [&]() {
      const u16* src = wo + ((size_t)(kvh * 512 + sp * 128)) * D + lane * 8;
#pragma unroll 1
      for (int it = 0; it < 112; it += WOB) {
#pragma unroll
        for (int k = 0; k < WOB; ++k)
          __builtin_amdgcn_global_load_lds((const unsigned*)(src + (size_t)(it + k) * 512), (unsigned*)(lds_ + L_WO + (it + k) * 1024), 16, 0, AUXPOL);
        WAITV(0);
      }
    };
    const int rs = lane / 7, cc = lane - rs * 7;
    const __amdgpu_buffer_rsrc_t r1 = __builtin_amdgcn_make_buffer_rsrc((void*)w1, 0, D * F * 2, 0x00020000);
    const __amdgpu_buffer_rsrc_t r2 = __builtin_amdgcn_make_buffer_rsrc((void*)(w2 + (size_t)j32 * 56 * D), 0, 56 * 896, 0x00020000);
    const unsigned o1 = lane < 63 ? (unsigned)(rs * (F * 2) + j32 * 112 + cc * 16) : OOB;
    v4u wv[10];
#pragma unroll
    for (int k = 0; k < 10; ++k) wv[k] = __builtin_amdgcn_raw_buffer_load_b128(r1, o1, (unsigned)(k * 9 * F * 2), AUXPOL);
    const float b1v = bf2f(b1[j32 * 56 + (lane < 56 ? lane : 55)]);
    BAR();
    BAR();
    tag = *(const LAS unsigned*)(lds + L_TAG);
    const LAS u16* xn = (const LAS u16*)(lds + L_XN + b * 896);
    float acc[8] = {0, 0, 0, 0, 0, 0, 0, 0};
    const int rsc = rs < 9 ? rs : 8;
#pragma unroll 1
    for (int k0 = 0; k0 < 50; k0 += 10) {
#pragma unroll
      for (int k = 0; k < 10; ++k) {
        const int row = (k0 + k) * 9 + rsc;
        const float xv = bf2f(xn[row < D ? row : D - 1]);
#pragma unroll
        for (int e = 0; e < 4; ++e) { acc[2 * e] += xv * bflo(wv[k][e]); acc[2 * e + 1] += xv * bfhi(wv[k][e]); }
      }
      SB();
#pragma unroll
      for (int k = 0; k < 10; ++k) wv[k] = __builtin_amdgcn_raw_buffer_load_b128(r1, o1, (unsigned)((k0 + 10 + k) * 9 * F * 2), AUXPOL);
      SB();
    }
    const unsigned o2 = lane < 56 ? (unsigned)(lane * 16) : OOB;
    v4u w2v[14];
#pragma unroll
    for (int k = 0; k < 14; ++k) w2v[k] = __builtin_amdgcn_raw_buffer_load_b128(r2, o2, (unsigned)(k * 896), AUXPOL);
    LAS float* hred = (LAS float*)(lds + L_HRED);
    if (lane < 63) { *(LAS f32x4*)(hred + rs * 56 + cc * 8) = f32x4{acc[0], acc[1], acc[2], acc[3]}; *(LAS f32x4*)(hred + rs * 56 + cc * 8 + 4) = f32x4{acc[4], acc[5], acc[6], acc[7]}; }
    WAITL();
    LAS float* hs = (LAS float*)(lds + L_HS);
    if (lane < 56) {
      float s = 0.f;
#pragma unroll
      for (int r = 0; r < 9; ++r) s += hred[r * 56 + lane];
      const float t = rbf(rbf(s) + b1v);
      const float u = 0.7978845608028654f * (t + 0.044715f * t * t * t);
      const float e2 = __builtin_amdgcn_exp2f(u * (2.0f * L2E));
      const float th = 1.0f - 2.0f / (e2 + 1.0f);
      hs[lane] = rbf(0.5f * t * (1.0f + th));
    }
    WAITL();
    float a2[8] = {0, 0, 0, 0, 0, 0, 0, 0};
#pragma unroll 1
    for (int k0 = 0; k0 < 56; k0 += 14) {
#pragma unroll
      for (int k = 0; k < 14; ++k) {
        const float hv = hs[k0 + k];
#pragma unroll
        for (int e = 0; e < 4; ++e) { a2[2 * e] += hv * bflo(w2v[k][e]); a2[2 * e + 1] += hv * bfhi(w2v[k][e]); }
      }
      SB();
#pragma unroll
      for (int k = 0; k < 14; ++k) w2v[k] = __builtin_amdgcn_raw_buffer_load_b128(r2, o2, (unsigned)((k0 + 14 + k) * 896), AUXPOL);
      SB();
    }
    {
      LAS float* tr = (LAS float*)(lds + L_HRED);
      if (lane < 56) { *(LAS f32x4*)(tr + lane * 8) = f32x4{a2[0], a2[1], a2[2], a2[3]}; *(LAS f32x4*)(tr + lane * 8 + 4) = f32x4{a2[4], a2[5], a2[6], a2[7]}; }
      WAITL();
      const int ci = lane & 15, cic = ci < 14 ? ci : 13;
      float v[8];
#pragma unroll
      for (int k = 0; k < 8; ++k) v[k] = tr[((lane >> 4) + 4 * k) * 14 + cic];
#pragma unroll
      for (int k = 0; k < 8; ++k) st_gran((GAS u64*)(wsg + WS_FH + (((b * 32 + (lane >> 4) + 4 * k) * 32 + j32) * 16 + ci) * 8), tag, __float_as_uint(v[k]));
    }
    wo_dma();
  }
  BAR();
  const int pair = b * 8 + kvh;
  if (w < 8) {
    const int h = tid >> 8;
    const LAS float* ml = (const LAS float*)(lds + L_ML);
    float M = -INFINITY;
#pragma unroll
    for (int ww = 0; ww < 8; ++ww) M = fmaxf(M, ml[ww * 4 + h * 2]);
    float L = 0.f, o = 0.f;
    const LAS float* ac = (const LAS float*)(lds + L_SC) + tid;
#pragma unroll
    for (int ww = 0; ww < 8; ++ww) { const float sc = __builtin_amdgcn_exp2f((ml[ww * 4 + h * 2] - M) * L2E); L += sc * ml[ww * 4 + h * 2 + 1]; o += sc * ac[ww * SC_STRIDE]; }
    const int dst = tid >> 7, idx = tid & 127;
    if (dst != sp) {
      GAS u64* pb = (GAS u64*)(wsg + WS_PBOX + ((pair * 4 + dst) * 4 + sp) * PMSG);
      st_gran(pb + idx, tag, __float_as_uint(o));
      if (idx == 0) { st_gran(pb + 128, tag, __float_as_uint(M)); st_gran(pb + 129, tag, __float_as_uint(L)); }
    } else {
      GAS u64* pb = (GAS u64*)(wsg + WS_PBOX + ((pair * 4 + sp) * 4) * PMSG);
      float ov[3], mv[3], lv[3];
      for (;;) {
        asm volatile("" ::: "memory");
        u64 gv[3], gm[3];
#pragma unroll
        for (int k = 0; k < 3; ++k) {
          const int src = (sp + 1 + k) & 3;
          gv[k] = ld_gran(pb + src * (PMSG / 8) + idx);
          gm[k] = ld_gran(pb + src * (PMSG / 8) + 128 + (lane & 1));
        }
        bool ok = true;
#pragma unroll
        for (int k = 0; k < 3; ++k) {
          ok &= ((unsigned)(gv[k] >> 32) == tag) && ((unsigned)(gm[k] >> 32) == tag);
          ov[k] = __uint_as_float((unsigned)gv[k]);
          mv[k] = __builtin_bit_cast(float, __builtin_amdgcn_readlane((int)(unsigned)gm[k], 0));
          lv[k] = __builtin_bit_cast(float, __builtin_amdgcn_readlane((int)(unsigned)gm[k], 1));
        }
        if (__all(ok)) break;
        if (rt() - tstart > TMO) break;
        __builtin_amdgcn_s_sleep(2);
      }
      const float Mx = fmaxf(fmaxf(M, mv[0]), fmaxf(mv[1], mv[2]));
      const float eo = __builtin_amdgcn_exp2f((M - Mx) * L2E), e0 = __builtin_amdgcn_exp2f((mv[0] - Mx) * L2E), e1 = __builtin_amdgcn_exp2f((mv[1] - Mx) * L2E), e2 = __builtin_amdgcn_exp2f((mv[2] - Mx) * L2E);
      const float Lt = eo * L + e0 * lv[0] + e1 * lv[1] + e2 * lv[2];
      const float of = (eo * o + e0 * ov[0] + e1 * ov[1] + e2 * ov[2]) / Lt;
      ((LAS float*)(lds + L_OS))[idx] = rbf(of);
    }
  }
  BAR();
  if (w < 8) {
    const int rg = lane & 7, cc = (lane >> 3) < 7 ? (lane >> 3) : 6;
    float a[8] = {0, 0, 0, 0, 0, 0, 0, 0};
    const LAS float* os = (const LAS float*)(lds + L_OS) + rg;
    const LAS unsigned char* wp = lds + L_WO + rg * 896 + (7 * w + cc) * 16;
#pragma unroll
    for (int r = 0; r < 16; ++r) {
      const v4u wv = *(const LAS v4u*)(wp + r * 8 * 896);
      const float ov = os[8 * r];
#pragma unroll
      for (int e = 0; e < 4; ++e) { a[2 * e] += ov * bflo(wv[e]); a[2 * e + 1] += ov * bfhi(wv[e]); }
    }
    float y[4];
#pragma unroll
    for (int v = 0; v < 4; ++v) { const float t1 = a[v] + dpp<HMIR>(a[v]), t2 = a[v + 4] + dpp<HMIR>(a[v + 4]); y[v] = (lane & 4) ? t2 : t1; }
    float z[2];
#pragma unroll
    for (int v = 0; v < 2; ++v) { const float t1 = y[v] + dpp<XOR2>(y[v]), t2 = y[v + 2] + dpp<XOR2>(y[v + 2]); z[v] = (lane & 2) ? t2 : t1; }
    const float t1 = z[0] + dpp<XOR1>(z[0]), t2 = z[1] + dpp<XOR1>(z[1]);
    const float s = (lane & 1) ? t2 : t1;
    const int ci = lane & 15, srcl = (lane >> 4) * 14 + (ci < 14 ? ci : 13);
    const float sv = __builtin_bit_cast(float, __builtin_amdgcn_ds_bpermute(srcl * 4, __builtin_bit_cast(int, s)));
    st_gran((GAS u64*)(wsg + WS_FS + (((b * 32 + 4 * w + (lane >> 4)) * 32 + j32) * 16 + ci) * 8), tag, __float_as_uint(sv));
  }
  if (w == 0) {
    GAS u64* fb = (GAS u64*)(wsg + WS_FS + (b * 32 + j32) * 4096) + lane;
    float s;
    for (;;) {
      asm volatile("" ::: "memory");
      u64 gv[8]; bool ok = true; s = 0.f;
#pragma unroll
      for (int k = 0; k < 8; ++k) gv[k] = ld_gran(fb + k * 64);
#pragma unroll
      for (int k = 0; k < 8; ++k) { ok &= ((unsigned)(gv[k] >> 32) == tag) || ((lane & 15) >= 14); s += __uint_as_float((unsigned)gv[k]); }
      if (__all(ok)) break;
      if (rt() - tstart > TMO) break;
      __builtin_amdgcn_s_sleep(2);
    }
    s = sum16(s); s = sum32(s);
    if (lane < 14) {
      const int col = j32 * 14 + lane;
      const float sg = ((const LAS float*)(lds + L_G))[lane], xv = ((const LAS float*)(lds + L_G))[16 + lane];
      const float r = xv + rbf(rbf(s) * sg);
      out[b * D + col] = (u16)(pk2(r, r) & 0xffffu);
    }
    if (bid == 0 && lane == 0) {
      const unsigned E = __hip_atomic_load((GAS unsigned*)(wsg + WS_EPOCH), RLX_AGENT);
      __hip_atomic_store((GAS unsigned*)(wsg + WS_EPOCH), E + 1u, RLX_AGENT);
    }
  }
}

extern "C" void kernel_launch(void* const* d_in, const int* in_sizes, int n_in, void* d_out, int out_size, void* d_ws, size_t ws_size, hipStream_t stream) {
  (void)hipFuncSetAttribute((const void*)fused, hipFuncAttributeMaxDynamicSharedMemorySize, L_END);
  hipLaunchKernelGGL(fused, dim3(256), dim3(NTHREADS), L_END, stream,
                     (const u16*)d_in[0], (const u16*)d_in[1], (const u16*)d_in[2], (const u16*)d_in[3], (const u16*)d_in[4],
                     (const u16*)d_in[6], (const u16*)d_in[7], (const u16*)d_in[8], (const u16*)d_in[9], (u16*)d_out, (unsigned char*)d_ws);
}
```
